```python
import torch

def _softmax(x, axis=-1):
    return torch.softmax(x, dim=axis)

def _gelu(x):
    return torch.nn.functional.gelu(x, approximate='tanh')

def _silu(x):
    return torch.nn.functional.silu(x)

def _sigmoid(x):
    return torch.sigmoid(x)

def _layernorm(x, axis=-1, eps=1e-05):
    m = x.mean(dim=axis, keepdim=True)
    v = x.var(dim=axis, keepdim=True, unbiased=False)
    return (x - m) / torch.sqrt(v + eps)

def _scatter_add(tgt, idx, src, axis=0):
    return tgt.clone().index_add_(axis, idx, src)

def _segment_sum(src, seg, n):
    out = torch.zeros((n,) + src.shape[1:], dtype=src.dtype)
    return out.index_add_(0, seg, src)

def _pad(x, pw):
    flat = tuple((v for pair in reversed(pw) for v in pair))
    return torch.nn.functional.pad(x, flat)
kernel_name = 'adaLN_cond__ssm_conv_h3__glu_gated_reglu__prompt'

def setup_inputs():

    _w = lambda a: (lambda t: t.to(torch.bfloat16) if t.is_floating_point() else t)(torch.as_tensor(a))

    def _A_setup():
        B, S, D = (1, 2048, 640)
        ks = 4
        return {'q': _w((lambda *s: torch.randn(*s, dtype=torch.bfloat16) * 0.05)(B, S, D)), 'k': _w((lambda *s: torch.randn(*s, dtype=torch.bfloat16) * 1.6)(B, S, D)), 'v': _w((lambda *s: torch.randn(*s, dtype=torch.bfloat16) * 1.6)(B, S, D)), 'shift_k': _w((lambda *s: torch.randn(*s, dtype=torch.bfloat16) * 1.6)(D, ks) * 0.5), 'ssm_k': _w((lambda *s: torch.randn(*s, dtype=torch.bfloat16) * 1.6)(D, S) * (1.0 / S ** 0.5)), 'ssm_D': _w((lambda *s: torch.randn(*s, dtype=torch.bfloat16) * 1.6)(D))}
    _A_in = _A_setup()

    def _B_setup():
        B, S, D, F = (1, 2048, 640, 1728)
        return {'x': _w((lambda *s: torch.randn(*s, dtype=torch.bfloat16) * 0.05)(B, S, D)), 'W_gate': _w((lambda *s: torch.randn(*s, dtype=torch.bfloat16) * 3.2)(D, F) * (2.0 / D) ** 0.5), 'W_up': _w((lambda *s: torch.randn(*s, dtype=torch.bfloat16) * 3.2)(D, F) * (2.0 / D) ** 0.5), 'W_down': _w((lambda *s: torch.randn(*s, dtype=torch.bfloat16) * 3.2)(F, D) * (1.0 / F) ** 0.5)}
    _B_in = _B_setup()
    return {'A_q': _A_in['q'], 'A_k': _A_in['k'], 'A_v': _A_in['v'], 'A_shift_k': _A_in['shift_k'], 'A_ssm_k': _A_in['ssm_k'], 'A_ssm_D': _A_in['ssm_D'], 'B_x': _B_in['x'], 'B_W_gate': _B_in['W_gate'], 'B_W_up': _B_in['W_up'], 'B_W_down': _B_in['W_down'], 'c': _w((lambda *s: torch.randn(*s, dtype=torch.bfloat16) * 0.1)(1, 640) * 8.0), 'w_mod': _w((lambda *s: torch.randn(*s, dtype=torch.bfloat16) * 0.1)(640, 6 * 640) * 640 ** (-0.5) * 8.0)}

def reference(A_q, A_k, A_v, A_shift_k, A_ssm_k, A_ssm_D, B_x, B_W_gate, B_W_up, B_W_down, c, w_mod):

    def _A_ref(q, k, v, shift_k, ssm_k, ssm_D):
        B, S, D = q.shape
        ks = shift_k.shape[-1]
        k_pad = _pad(k, ((0, 0), (ks - 1, 0), (0, 0)))
        k_shift = torch.zeros_like(k)
        for t in range(ks):
            k_shift = k_shift + k_pad[:, t:t + S, :] * shift_k[:, ks - 1 - t][None, None, :]
        kv = k_shift * v
        L = 2 * S
        kv_pad = _pad(kv, ((0, 0), (0, S), (0, 0)))
        kf_pad = _pad(ssm_k, ((0, 0), (0, S)))
        kv_f = (lambda x, axes=(-2, -1): torch.fft.rfft2(x.float(), dim=axes))(kv_pad[:, None, :, :], axes=(1, 2))
        kf_f = (lambda x, axes=(-2, -1): torch.fft.rfft2(x.float(), dim=axes))(kf_pad[:, None, :], axes=(1, 2))
        kf_f = (lambda x, *d: x.permute(*d))(kf_f, 1, 2, 0)
        y_f = kv_f * kf_f[None, :, :, :]
        y = (lambda x, s, axes=(-2, -1): torch.fft.irfft2(x, s=s, dim=axes).to(torch.bfloat16))(y_f, (1, L), axes=(1, 2))[:, 0, :S, :]
        y = y + kv * ssm_D[None, None, :]
        return q * y

    def _B_ref(x, W_gate, W_up, W_down):
        gate = torch.einsum('bsd,df->bsf', x, W_gate)
        up = torch.einsum('bsd,df->bsf', x, W_up)
        gate = gate * (lambda x, dt: x.to(dt))(gate > 0, gate.dtype)
        h = gate * up
        return torch.einsum('bsf,fd->bsd', h, W_down)
    x = A_q
    mod = (_silu(c) @ w_mod).reshape(c.shape[0], 6, 1, -1)
    sa, ca, ga, sb, cb, gb = (mod[:, 0].clone(), mod[:, 1].clone(), mod[:, 2].clone(), mod[:, 3].clone(), mod[:, 4].clone(), mod[:, 5].clone())
    h = _layernorm(x, axis=-1) * (1 + ca) + sa
    x = x + ga * _A_ref(h, A_k, A_v, A_shift_k, A_ssm_k, A_ssm_D)
    h = _layernorm(x, axis=-1) * (1 + cb) + sb
    x = x + gb * _B_ref(h, B_W_gate, B_W_up, B_W_down)
    return x
```

```cpp
#include <hip/hip_runtime.h>
#include <hip/hip_bf16.h>
#include <stdint.h>
typedef unsigned short u16;
constexpr int S=2048,D=640,F=1728;
__device__ __forceinline__ float b2f(u16 x){return __uint_as_float(((unsigned)x)<<16);}
__device__ __forceinline__ u16 f2b(float f){__hip_bfloat16 h=__float2bfloat16(f);return *(u16*)&h;}
__device__ __forceinline__ float rb(float f){return b2f(f2b(f));}

__global__ void k_mod(const u16* c,const u16* w,float* mod){
  int j=blockIdx.x*blockDim.x+threadIdx.x; if(j>=6*D) return;
  float a=0; for(int i=0;i<D;i++){float x=b2f(c[i]); float s=rb(x/(1.f+__expf(-x))); a+=s*b2f(w[(size_t)i*6*D+j]);}
  mod[j]=rb(a);
}
__global__ void k_kv(const u16* k,const u16* v,const u16* sh,u16* kvT){
  int idx=blockIdx.x*blockDim.x+threadIdx.x; if(idx>=S*D) return;
  int s=idx/D,d=idx%D; float a=0;
  for(int t=0;t<4;t++){int j=3-t; float kk=(s-j>=0)?b2f(k[(s-j)*D+d]):0.f; a=rb(a+rb(kk*b2f(sh[d*4+j])));}
  kvT[d*S+s]=f2b(a*b2f(v[idx]));
}
__global__ void k_conv(const u16* kvT,const u16* K,const u16* Dv,float* z){
  __shared__ float u[S],kk[S];
  int d=blockIdx.x;
  for(int i=threadIdx.x;i<S;i+=blockDim.x){u[i]=b2f(kvT[d*S+i]);kk[i]=b2f(K[d*S+i]);}
  __syncthreads();
  for(int s=threadIdx.x;s<S;s+=blockDim.x){float a=0; for(int t=0;t<=s;t++) a+=kk[t]*u[s-t];
    z[s*D+d]=rb(rb(a)+rb(u[s]*b2f(Dv[d])));}
}
__device__ float bsum(float x,float* sm){
  for(int o=32;o>0;o>>=1) x+=__shfl_xor(x,o);
  __syncthreads(); if((threadIdx.x&63)==0) sm[threadIdx.x>>6]=x; __syncthreads();
  float r=0; for(int i=0;i<(int)blockDim.x/64;i++) r+=sm[i]; return r;
}
__global__ void k_ln(const u16* q,const float* z,const float* mod,u16* x1,u16* h2){
  __shared__ float sm[16]; int s=blockIdx.x,d=threadIdx.x;
  float x=b2f(q[s*D+d]); float m=bsum(x,sm)/D; float v=bsum((x-m)*(x-m),sm)/D;
  float h=(x-m)*rsqrtf(v+1e-5f)*(1.f+mod[D+d])+mod[d];
  float y=rb(x+mod[2*D+d]*(h*z[s*D+d]));
  x1[s*D+d]=f2b(y);
  m=bsum(y,sm)/D; v=bsum((y-m)*(y-m),sm)/D;
  h2[s*D+d]=f2b((y-m)*rsqrtf(v+1e-5f)*(1.f+mod[4*D+d])+mod[3*D+d]);
}
__global__ void k_g1(const u16* h2,const u16* Wg,const u16* Wu,u16* act){
  __shared__ float a[D]; int s=blockIdx.x;
  for(int i=threadIdx.x;i<D;i+=blockDim.x) a[i]=b2f(h2[s*D+i]);
  __syncthreads();
  for(int f=threadIdx.x;f<F;f+=blockDim.x){float g=0,u=0; for(int i=0;i<D;i++){g+=a[i]*b2f(Wg[i*F+f]);u+=a[i]*b2f(Wu[i*F+f]);}
    g=rb(g);u=rb(u); act[s*F+f]=f2b((g>0?g:0.f)*u);}
}
__global__ void k_g2(const u16* act,const u16* Wd,const u16* x1,const float* mod,u16* out){
  __shared__ float a[F]; int s=blockIdx.x,d=threadIdx.x;
  for(int i=threadIdx.x;i<F;i+=blockDim.x) a[i]=b2f(act[s*F+i]);
  __syncthreads();
  float o=0; for(int i=0;i<F;i++) o+=a[i]*b2f(Wd[i*D+d]);
  out[s*D+d]=f2b(b2f(x1[s*D+d])+rb(mod[5*D+d]*rb(o)));
}
extern "C" void kernel_launch(void* const* d_in, const int* in_sizes, int n_in,
                              void* d_out, int out_size, void* d_ws, size_t ws_size,
                              hipStream_t stream) {
  const u16* q=(const u16*)d_in[0],*k=(const u16*)d_in[1],*v=(const u16*)d_in[2],*sh=(const u16*)d_in[3],*K=(const u16*)d_in[4],*Dv=(const u16*)d_in[5];
  const u16* Wg=(const u16*)d_in[7],*Wu=(const u16*)d_in[8],*Wd=(const u16*)d_in[9],*c=(const u16*)d_in[10],*wm=(const u16*)d_in[11];
  char* ws=(char*)d_ws;
  float* mod=(float*)ws; u16* kvT=(u16*)(ws+(1<<20)); float* z=(float*)(ws+(8<<20)); u16* x1=(u16*)(ws+(16<<20)); u16* h2=(u16*)(ws+(24<<20)); u16* act=(u16*)(ws+(32<<20));
  k_mod<<<(6*D+63)/64,64,0,stream>>>(c,wm,mod);
  k_kv<<<S*D/256,256,0,stream>>>(k,v,sh,kvT);
  k_conv<<<D,256,0,stream>>>(kvT,K,Dv,z);
  k_ln<<<S,D,0,stream>>>(q,z,mod,x1,h2);
  k_g1<<<S,256,0,stream>>>(h2,Wg,Wu,act);
  k_g2<<<S,D,0,stream>>>(act,Wd,x1,mod,(u16*)d_out);
}
```

```cpp
#include <hip/hip_runtime.h>
#include <stdint.h>

typedef unsigned short u16;
typedef unsigned int u32;
typedef unsigned long long u64;
typedef __attribute__((ext_vector_type(8))) __bf16 bf16x8;
typedef __attribute__((ext_vector_type(2))) __bf16 bf16x2;
typedef __attribute__((ext_vector_type(2))) float f32x2;
typedef __attribute__((ext_vector_type(4))) float f32x4;
typedef __attribute__((ext_vector_type(4))) u32 u32x4;
typedef __attribute__((ext_vector_type(2))) u32 u32x2;
typedef __attribute__((ext_vector_type(4))) short s16x4;
#define LDS __attribute__((address_space(3)))
#define DEV __device__ __forceinline__
#define SB() __builtin_amdgcn_sched_barrier(0)

constexpr int S = 2048, D = 640, F = 1728;
constexpr size_t WS_MOD = 0;
constexpr size_t WS_ZT  = 64 << 10;
constexpr size_t WS_X1  = WS_ZT + (size_t)S * D * 2;
constexpr size_t WS_H2  = WS_X1 + (size_t)S * D * 2;
constexpr size_t WS_W1  = WS_H2 + (size_t)S * D * 2;
constexpr size_t WS_W2  = WS_W1 + (size_t)2 * D * F * 2;
constexpr size_t WS_ACT = WS_W2 + (size_t)D * F * 2;
constexpr size_t WS_END = WS_ACT + (size_t)S * F * 2;
constexpr size_t WS_STAMP = 64 << 20;

#define AUX_IN 2

#define STAMP(k, i)

DEV float blo(u32 x) { return __uint_as_float(x << 16); }
DEV float bhi(u32 x) { return __uint_as_float(x & 0xffff0000u); }
DEV u32 pk2(float a, float b) { f32x2 v = {a, b}; return __builtin_bit_cast(u32, __builtin_convertvector(v, bf16x2)); }
DEV f32x4 mfma16(u32x4 a, u32x4 b, f32x4 c) {
  return __builtin_amdgcn_mfma_f32_16x16x32_bf16(__builtin_bit_cast(bf16x8, a), __builtin_bit_cast(bf16x8, b), c, 0, 0, 0);
}
DEV float silu_bf(u32 c) { float x = blo(c); return blo(pk2(x / (1.f + __expf(-x)), 0.f)); }
DEV __amdgpu_buffer_rsrc_t rsrc(const void* p, u32 bytes) { return __builtin_amdgcn_make_buffer_rsrc((void*)p, 0, bytes, 0x00020000); }
DEV float shr1(float x) { return __int_as_float(__builtin_amdgcn_update_dpp(0, __float_as_int(x), 0x138, 0xf, 0xf, true)); }
DEV u32x2 trread(LDS char* p) { return __builtin_bit_cast(u32x2, __builtin_amdgcn_ds_read_tr16_b64_v4i16((LDS s16x4*)p)); }
DEV void bar() { asm volatile("s_waitcnt lgkmcnt(0)" ::: "memory"); __builtin_amdgcn_s_barrier(); asm volatile("" ::: "memory"); }

constexpr int KC_COPY = 4160;
constexpr int UR_BYTES = 4608;
constexpr int K1_LDS = 4 * 2 * KC_COPY + 4 * UR_BYTES;

DEV void retile_unit(u32x4 (&v)[4], LDS char* buf, char* dst, int fragstride, bool kperm, int lane) {
#pragma unroll
  for (int i = 0; i < 4; i++) {
    int c = lane + 64 * i, r = c >> 3, ch = c & 7;
    int h = ((r >> 1) & 1) | ((((r >> 2) ^ (r >> 3)) & 1) << 1);
    *(LDS u32x4*)(buf + r * 128 + ((ch ^ (h << 1)) << 4)) = v[i];
  }
  int g = lane >> 4, q = (lane >> 2) & 3, p = lane & 3;
  int r0 = kperm ? 4 * g + q : 8 * g + q, r1 = kperm ? r0 + 16 : r0 + 4;
  int h0 = ((r0 >> 1) & 1) | ((((r0 >> 2) ^ (r0 >> 3)) & 1) << 1);
  int h1 = ((r1 >> 1) & 1) | ((((r1 >> 2) ^ (r1 >> 3)) & 1) << 1);
#pragma unroll
  for (int t = 0; t < 4; t++) {
    u32x2 a = trread(buf + r0 * 128 + ((t ^ h0) << 5) + p * 8);
    u32x2 b = trread(buf + r1 * 128 + ((t ^ h1) << 5) + p * 8);
    u32x4 o = {a.x, a.y, b.x, b.y};
    *(u32x4*)(dst + (size_t)t * fragstride + lane * 16) = o;
  }
}

__global__ __launch_bounds__(256, 1) void k1(const u16* __restrict__ kk, const u16* __restrict__ vv, const u16* __restrict__ shk,
                                             const u16* __restrict__ ssk, const u16* __restrict__ ssd, const u16* __restrict__ cc,
                                             const u16* __restrict__ wmod, const u16* __restrict__ wg, const u16* __restrict__ wu,
                                             const u16* __restrict__ wd, char* __restrict__ ws) {
  __shared__ __attribute__((aligned(16))) char smem[K1_LDS];
  LDS char* sm = (LDS char*)smem;
  const int tid = threadIdx.x, lane = tid & 63, w = __builtin_amdgcn_readfirstlane(tid >> 6);
  const int b = blockIdx.x, xcd = b & 7, bi = b >> 3;
  STAMP(0, 0);
  auto rk = rsrc(kk, S * D * 2), rv = rsrc(vv, S * D * 2);
  u32 pf;
  {
    int row = 64 * bi + (tid >> 2), off = row * 1280 + ((160 * xcd) & ~127) + ((tid >> 1) & 1) * 128;
    const u16* base = (tid & 1) ? vv : kk;
    pf = __builtin_nontemporal_load((const u32*)((const char*)base + off));
  }
  if (bi < 20) {
    const int cg = xcd * 20 + bi, d = cg * 4 + w;
    auto rs = rsrc(ssk, S * D * 2);
    u32x4 kw[4];
#pragma unroll
    for (int i = 0; i < 4; i++) kw[i] = __builtin_amdgcn_raw_buffer_load_b128(rs, d * 4096 + (lane + 64 * i) * 16, 0, AUX_IN);
    u32x2 gk[9], gv[9];
#pragma unroll
    for (int r = 0; r < 9; r++) {
      int row = 61 * (4 * r + w) - 3 + lane;
      int off = row * 1280 + cg * 8;
      gk[r] = __builtin_amdgcn_raw_buffer_load_b64(rk, off, 0, AUX_IN);
      gv[r] = __builtin_amdgcn_raw_buffer_load_b64(rv, off, 0, AUX_IN);
      SB();
    }
    float cf[4][4];
    {
      const u32x4* sp = (const u32x4*)(shk + cg * 16);
      u32x4 s0 = sp[0], s1 = sp[1];
      cf[0][0] = blo(s0.x); cf[0][1] = bhi(s0.x); cf[0][2] = blo(s0.y); cf[0][3] = bhi(s0.y);
      cf[1][0] = blo(s0.z); cf[1][1] = bhi(s0.z); cf[1][2] = blo(s0.w); cf[1][3] = bhi(s0.w);
      cf[2][0] = blo(s1.x); cf[2][1] = bhi(s1.x); cf[2][2] = blo(s1.y); cf[2][3] = bhi(s1.y);
      cf[3][0] = blo(s1.z); cf[3][1] = bhi(s1.z); cf[3][2] = blo(s1.w); cf[3][3] = bhi(s1.w);
    }
    const u32 ddw = ((const u32*)ssd)[d >> 1];
    const float dd = (d & 1) ? bhi(ddw) : blo(ddw);
    LDS char* kc = sm + w * 2 * KC_COPY;
    LDS char* urb = sm + 4 * 2 * KC_COPY;
    LDS char* ur = urb + w * UR_BYTES;
    const u32x4 z4 = {0, 0, 0, 0};
    if (lane < 32) *(LDS u32x4*)(ur + 4096 + lane * 16) = z4;
#pragma unroll
    for (int i = 0; i < 4; i++) {
      u32 nx0 = i < 3 ? __builtin_amdgcn_readfirstlane(kw[i < 3 ? i + 1 : i].x) : 0u;
      u32 nx = __builtin_amdgcn_update_dpp(nx0, kw[i].x, 0x130, 0xf, 0xf, false);
      u32x4 o = {__builtin_amdgcn_alignbit(kw[i].y, kw[i].x, 16), __builtin_amdgcn_alignbit(kw[i].z, kw[i].y, 16),
                 __builtin_amdgcn_alignbit(kw[i].w, kw[i].z, 16), __builtin_amdgcn_alignbit(nx, kw[i].w, 16)};
      *(LDS u32x4*)(kc + 32 + (lane + 64 * i) * 16) = kw[i];
      *(LDS u32x4*)(kc + KC_COPY + 32 + (lane + 64 * i) * 16) = o;
    }
    if (lane == 0) {
      u32x4 e = {0, 0, 0, kw[0].x << 16};
      *(LDS u32x4*)(kc) = z4; *(LDS u32x4*)(kc + 16) = z4;
      *(LDS u32x4*)(kc + KC_COPY) = z4; *(LDS u32x4*)(kc + KC_COPY + 16) = e;
    }
    const int a = lane & 15, g = lane >> 4;
    u32x4 A[64];
    {
      LDS const u32* ap = (LDS const u32*)(kc + ((a & 1) ? 0 : KC_COPY)) + 4 * g + ((a + 1) >> 1);
#pragma unroll
      for (int m = 0; m < 64; m++) { A[m].x = ap[16 * m]; A[m].y = ap[16 * m + 1]; A[m].z = ap[16 * m + 2]; A[m].w = ap[16 * m + 3]; }
    }
    STAMP(0, 1);
    f32x4 acc[8];
#pragma unroll
    for (int n = 0; n < 8; n++) acc[n] = (f32x4){0, 0, 0, 0};
    LDS char* bp = ur + (2032 - 16 * a + 8 * g) * 2;
    char* zt = ws + WS_ZT + (size_t)d * 4096 + (16 * a + 4 * g) * 2;
#pragma unroll
    for (int r = 0; r < 9; r++) {
      int row = 61 * (4 * r + w) - 3 + lane;
      float kf[4] = {blo(gk[r].x), bhi(gk[r].x), blo(gk[r].y), bhi(gk[r].y)};
      float vf[4] = {blo(gv[r].x), bhi(gv[r].x), blo(gv[r].y), bhi(gv[r].y)};
      float u[4];
#pragma unroll
      for (int c = 0; c < 4; c++) {
        float t = kf[c] * cf[c][3];
        t = shr1(t) + kf[c] * cf[c][2];
        t = shr1(t) + kf[c] * cf[c][1];
        t = shr1(t) + kf[c] * cf[c][0];
        u[c] = t * vf[c];
      }
      u32 p01 = pk2(u[0], u[1]), p23 = pk2(u[2], u[3]);
      if (lane >= 3 && row < S) {
        LDS u16* up = (LDS u16*)(urb + (2047 - row) * 2);
        up[0] = (u16)p01; up[UR_BYTES / 2] = (u16)(p01 >> 16);
        up[UR_BYTES] = (u16)p23; up[3 * UR_BYTES / 2] = (u16)(p23 >> 16);
      }
      bar();
      if (r >= 1) {
        const int y = r - 1;
#pragma unroll
        for (int rho = 0; rho < 8; rho++) {
          u32x4 B = *(LDS u32x4*)(bp + (32 * rho - 256 * y) * 2);
#pragma unroll
          for (int x = 0; x < 8 - y; x++) acc[x + y] = mfma16(A[8 * x + rho], B, acc[x + y]);
        }
        u32x2 uu = *(LDS u32x2*)(ur + (2044 - 256 * y - 16 * a - 4 * g) * 2);
        u32x2 o = {pk2(acc[y][0] + bhi(uu.y) * dd, acc[y][1] + blo(uu.y) * dd), pk2(acc[y][2] + bhi(uu.x) * dd, acc[y][3] + blo(uu.x) * dd)};
        *(u32x2*)(zt + 512 * y) = o;
      }
      if (r == 1) { STAMP(0, 2); }
    }
    asm volatile("" ::"v"(pf));
    STAMP(0, 3);
    return;
  }
  {
    const int sid = (bi - 20) * 8 + xcd;
    int u0, nu;
    if (sid < 60) {
      u0 = sid * 9; nu = 9;
      auto rw = rsrc(wmod, D * 6 * D * 2);
      const int ch = tid & 7, rg = tid >> 3;
      u32x4 wv[20];
      u16 c0 = cc[tid], c1 = cc[256 + tid], c2 = cc[512 + (tid & 127)];
      SB();
#pragma unroll
      for (int i = 0; i < 20; i++) wv[i] = __builtin_amdgcn_raw_buffer_load_b128(rw, (rg + 32 * i) * 7680 + sid * 128 + ch * 16, 0, AUX_IN);
      LDS float* sil = (LDS float*)(sm + 20480);
      SB();
      sil[tid] = silu_bf(c0); sil[256 + tid] = silu_bf(c1); sil[512 + (tid & 127)] = silu_bf(c2);
      bar();
      float ac[8] = {0, 0, 0, 0, 0, 0, 0, 0};
#pragma unroll
      for (int i = 0; i < 20; i++) {
        float s = sil[rg + 32 * i];
        ac[0] += s * blo(wv[i].x); ac[1] += s * bhi(wv[i].x); ac[2] += s * blo(wv[i].y); ac[3] += s * bhi(wv[i].y);
        ac[4] += s * blo(wv[i].z); ac[5] += s * bhi(wv[i].z); ac[6] += s * blo(wv[i].w); ac[7] += s * bhi(wv[i].w);
      }
#pragma unroll
      for (int j = 0; j < 8; j++) { ac[j] += __shfl_xor(ac[j], 8); ac[j] += __shfl_xor(ac[j], 16); ac[j] += __shfl_xor(ac[j], 32); }
      LDS float* red = (LDS float*)(sm + 24576);
      if (lane < 8) {
#pragma unroll
        for (int j = 0; j < 8; j++) red[w * 64 + lane * 8 + j] = ac[j];
      }
      bar();
      if (tid < 64) ((u16*)(ws + WS_MOD))[sid * 64 + tid] = (u16)pk2(red[tid] + red[64 + tid] + red[128 + tid] + red[192 + tid], 0.f);
    } else {
      u0 = 540 + (sid - 60) * 30; nu = 30;
    }
    STAMP(0, 1);
    LDS char* buf = sm + w * 4096;
    for (int j0 = w; j0 < nu; j0 += 16) {
      u32x4 v[4][4];
#pragma unroll
      for (int jj = 0; jj < 4; jj++) {
        int u = u0 + min(j0 + 4 * jj, nu - 1);
        const char* src; int pitch;
        if (u < 1080) { int mat = u >= 540, uu = u - 540 * mat, ks = uu / 27, fq = uu - 27 * ks; src = (const char*)(mat ? wu : wg) + ks * 32 * 3456 + fq * 128; pitch = 3456; }
        else { int uu = u - 1080, ks = uu / 10, nq = uu - 10 * ks; src = (const char*)wd + ks * 32 * 1280 + nq * 128; pitch = 1280; }
#pragma unroll
        for (int i = 0; i < 4; i++) { int c = lane + 64 * i; v[jj][i] = __builtin_nontemporal_load((const u32x4*)(src + (c >> 3) * pitch + (c & 7) * 16)); }
      }
#pragma unroll
      for (int jj = 0; jj < 4; jj++) {
        if (j0 + 4 * jj < nu) {
          int u = u0 + j0 + 4 * jj;
          if (u < 1080) { int mat = u >= 540, uu = u - 540 * mat, ks = uu / 27, fq = uu - 27 * ks;
            retile_unit(v[jj], buf, ws + WS_W1 + ((size_t)(mat * 108 + fq * 4) * 20 + ks) * 1024, 20 * 1024, false, lane); }
          else { int uu = u - 1080, ks = uu / 10, nq = uu - 10 * ks;
            retile_unit(v[jj], buf, ws + WS_W2 + ((size_t)(nq * 4) * 54 + ks) * 1024, 54 * 1024, true, lane); }
        }
      }
    }
  }
  asm volatile("" ::"v"(pf));
  STAMP(0, 3);
}

DEV void red8(float& s, float& q) {
  s += __shfl_xor(s, 8); q += __shfl_xor(q, 8);
  s += __shfl_xor(s, 16); q += __shfl_xor(q, 16);
  s += __shfl_xor(s, 32); q += __shfl_xor(q, 32);
}
DEV void unpack8(u32x4 v, float (&f)[8]) {
  f[0] = blo(v.x); f[1] = bhi(v.x); f[2] = blo(v.y); f[3] = bhi(v.y); f[4] = blo(v.z); f[5] = bhi(v.z); f[6] = blo(v.w); f[7] = bhi(v.w);
}
__global__ __launch_bounds__(640) void k2(const u16* __restrict__ qq, char* __restrict__ ws) {
  __shared__ __attribute__((aligned(16))) char smem[10240 + 2 * 640];
  LDS char* sm = (LDS char*)smem;
  const int tid = threadIdx.x, lane = tid & 63, w = tid >> 6, t = tid & 7, c = tid >> 3;
  const int s0 = ((blockIdx.x & 7) * 32 + (blockIdx.x >> 3)) * 8, s = s0 + t;
  STAMP(1, 0);
  u32x4 zr = *(const u32x4*)(ws + WS_ZT + (size_t)tid * 4096 + s0 * 2);
  u32x4 qv = __builtin_nontemporal_load((const u32x4*)(qq + s * D + 8 * c));
  const u32x4* mp = (const u32x4*)(ws + WS_MOD) + c;
  u32x4 msa = mp[0], mca = mp[80], mga = mp[160], msb = mp[240], mcb = mp[320];
  float x[8], z[8], p[8], o[8];
  unpack8(qv, x);
  float su = 0, sq = 0;
#pragma unroll
  for (int j = 0; j < 8; j++) { su += x[j]; sq += x[j] * x[j]; }
  red8(su, sq);
  LDS f32x2* red = (LDS f32x2*)(sm + 10240);
  if (lane < 8) red[w * 8 + lane] = (f32x2){su, sq};
  *(LDS u32x4*)(sm + tid * 16) = zr;
  bar();
  {
    int i = lane & 15, G = lane >> 4, q = i >> 2, pp = i & 3;
    LDS char* a = sm + w * 1024 + (16 * G + 8 * (pp >> 1) + q) * 16 + (pp & 1) * 8;
    u32x2 z0 = trread(a), z1 = trread(a + 64);
    unpack8((u32x4){z0.x, z0.y, z1.x, z1.y}, z);
  }
  su = 0; sq = 0;
#pragma unroll
  for (int i = 0; i < 10; i++) { f32x2 r = red[i * 8 + t]; su += r.x; sq += r.y; }
  float mean = su * (1.f / D), rstd = rsqrtf(fmaxf(sq * (1.f / D) - mean * mean, 0.f) + 1e-5f);
  float sa[8], ca[8], ga[8];
  unpack8(msa, sa); unpack8(mca, ca); unpack8(mga, ga);
#pragma unroll
  for (int j = 0; j < 8; j++) { float h = (x[j] - mean) * rstd * (1.f + ca[j]) + sa[j]; p[j] = x[j] + ga[j] * (h * z[j]); }
  u32x4 x1 = {pk2(p[0], p[1]), pk2(p[2], p[3]), pk2(p[4], p[5]), pk2(p[6], p[7])};
  *(u32x4*)(ws + WS_X1 + ((size_t)s * D + 8 * c) * 2) = x1;
  unpack8(x1, p);
  su = 0; sq = 0;
#pragma unroll
  for (int j = 0; j < 8; j++) { su += p[j]; sq += p[j] * p[j]; }
  red8(su, sq);
  if (lane < 8) red[80 + w * 8 + lane] = (f32x2){su, sq};
  bar();
  su = 0; sq = 0;
#pragma unroll
  for (int i = 0; i < 10; i++) { f32x2 r = red[80 + i * 8 + t]; su += r.x; sq += r.y; }
  mean = su * (1.f / D); rstd = rsqrtf(fmaxf(sq * (1.f / D) - mean * mean, 0.f) + 1e-5f);
  unpack8(msb, sa); unpack8(mcb, ca);
#pragma unroll
  for (int j = 0; j < 8; j++) o[j] = (p[j] - mean) * rstd * (1.f + ca[j]) + sa[j];
  u32x4 h2 = {pk2(o[0], o[1]), pk2(o[2], o[3]), pk2(o[4], o[5]), pk2(o[6], o[7])};
  *(u32x4*)(ws + WS_H2 + ((size_t)((s >> 4) * 20 + (c >> 2)) * 64 + (c & 3) * 16 + (s & 15)) * 16) = h2;
  STAMP(1, 3);
}

constexpr int G1_STAGE = 44 * 1024;
DEV void glds16(const char* g, LDS char* l) { __builtin_amdgcn_global_load_lds((const __attribute__((address_space(1))) void*)g, (LDS void*)l, 16, 0, 0); }

__global__ __launch_bounds__(256, 2) void k3(char* __restrict__ ws) {
  extern __shared__ __attribute__((aligned(16))) char smem[];
  LDS char* sm = (LDS char*)smem;
  const int tid = threadIdx.x, lane = tid & 63, w = __builtin_amdgcn_readfirstlane(tid >> 6);
  const int xcd = blockIdx.x & 7, bi = blockIdx.x >> 3;
  const int mt0 = (8 * (xcd >> 2) + (bi & 7)) * 8, fs = 4 * (xcd & 3) + (bi >> 3), ft0 = (27 * fs) >> 2;
  const int wm = w & 1, wn = w >> 1;
  STAMP(2, 0);
  const char* src[11];
#pragma unroll
  for (int j = 0; j < 11; j++) {
    int f = 11 * w + j, ks = f >= 22, e = f - 22 * ks;
    size_t off = e < 8 ? WS_H2 + (size_t)((mt0 + e) * 20 + ks) * 1024
                       : WS_W1 + (size_t)(((e >= 15 ? 108 - 15 : -8) + e + ft0) * 20 + ks) * 1024;
    src[j] = ws + off + lane * 16;
  }
  auto issue = [&](int st, int slot) {
#pragma unroll
    for (int j = 0; j < 11; j++) glds16(src[j] + st * 2048, sm + slot * G1_STAGE + (11 * w + j) * 1024);
  };
  f32x4 acc[7][4];
#pragma unroll
  for (int t = 0; t < 7; t++)
#pragma unroll
    for (int m = 0; m < 4; m++) acc[t][m] = (f32x4){0, 0, 0, 0};
  u32x4 H0[4], W0[7], H1[4], W1[7];
  LDS char* hb = sm + (4 * wm) * 1024 + lane * 16;
  LDS char* wb = sm + (8 + 7 * wn) * 1024 + lane * 16;
  auto rd = [&](u32x4 (&H)[4], u32x4 (&W)[7], int off) {
#pragma unroll
    for (int m = 0; m < 4; m++) H[m] = *(LDS u32x4*)(hb + off + m * 1024);
#pragma unroll
    for (int t = 0; t < 7; t++) W[t] = *(LDS u32x4*)(wb + off + t * 1024);
  };
  auto mm = [&](u32x4 (&H)[4], u32x4 (&W)[7]) {
#pragma unroll
    for (int t = 0; t < 7; t++)
#pragma unroll
      for (int m = 0; m < 4; m++) acc[t][m] = mfma16(W[t], H[m], acc[t][m]);
  };
  issue(0, 0); issue(1, 1); issue(2, 2);
  asm volatile("s_waitcnt vmcnt(22)" ::: "memory");
  __builtin_amdgcn_s_barrier();
  asm volatile("" ::: "memory");
  STAMP(2, 1);
  rd(H0, W0, 0);
  int slot = 0;
#pragma unroll 1
  for (int st = 0; st < 9; st++) {
    int so = slot * G1_STAGE;
    rd(H1, W1, so + 22 * 1024);
    mm(H0, W0);
    if (st < 8) asm volatile("s_waitcnt vmcnt(11) lgkmcnt(0)" ::: "memory");
    else asm volatile("s_waitcnt vmcnt(0) lgkmcnt(0)" ::: "memory");
    __builtin_amdgcn_s_barrier();
    asm volatile("" ::: "memory");
    if (st < 7) issue(st + 3, slot);
    slot = slot == 2 ? 0 : slot + 1;
    rd(H0, W0, slot * G1_STAGE);
    mm(H1, W1);
  }
  rd(H1, W1, slot * G1_STAGE + 22 * 1024);
  mm(H0, W0);
  mm(H1, W1);
  STAMP(2, 2);
  bar();
  LDS char* xo = sm + w * 8192 + lane * 8;
  LDS char* xi = sm + (w ^ 2) * 8192 + lane * 8;
#pragma unroll
  for (int t = 0; t < 7; t++)
#pragma unroll
    for (int m = 0; m < 2; m++) {
      f32x4 a = wn ? acc[t][m] : acc[t][2 + m];
      *(LDS u32x2*)(xo + (t * 2 + m) * 512) = (u32x2){pk2(a[0], a[1]), pk2(a[2], a[3])};
    }
  bar();
  const int nft = (fs & 3) == 0 ? 6 : 7;
#pragma unroll
  for (int t = 0; t < 7; t++)
#pragma unroll
    for (int m = 0; m < 2; m++) {
      f32x4 a = wn ? acc[t][2 + m] : acc[t][m];
      u32x2 own = {pk2(a[0], a[1]), pk2(a[2], a[3])};
      u32x2 oth = *(LDS u32x2*)(xi + (t * 2 + m) * 512);
      u32x2 gt = wn ? oth : own, up = wn ? own : oth;
      u32x2 o = {pk2(fmaxf(blo(gt.x), 0.f) * blo(up.x), fmaxf(bhi(gt.x), 0.f) * bhi(up.x)),
                 pk2(fmaxf(blo(gt.y), 0.f) * blo(up.y), fmaxf(bhi(gt.y), 0.f) * bhi(up.y))};
      int ft = ft0 + t, mt = mt0 + 4 * wm + 2 * wn + m;
      if (t < nft) *(u32x2*)(ws + WS_ACT + ((size_t)(mt * 54 + (ft >> 1)) * 64 + lane) * 16 + (ft & 1) * 8) = o;
    }
  STAMP(2, 3);
}

__global__ __launch_bounds__(256, 2) void k4(char* __restrict__ ws, u16* __restrict__ out) {
  __shared__ __attribute__((aligned(16))) char smem[4 * 15 * 1024];
  LDS char* sm = (LDS char*)smem;
  const int tid = threadIdx.x, lane = tid & 63, w = __builtin_amdgcn_readfirstlane(tid >> 6);
  const int xcd = blockIdx.x & 7, bi = blockIdx.x >> 3;
  const int mt0 = (8 * (xcd >> 1) + (bi & 7)) * 4, nt0 = (4 * (xcd & 1) + (bi >> 3)) * 5;
  STAMP(3, 0);
  const int ks0 = w < 2 ? 14 * w : 28 + 13 * (w - 2), nks = w < 2 ? 14 : 13;
  auto ra = rsrc(ws + WS_ACT + (size_t)(mt0 * 54 + ks0) * 1024, 4 * 54 * 1024);
  auto rw = rsrc(ws + WS_W2 + (size_t)(nt0 * 54 + ks0) * 1024, 5 * 54 * 1024);
  constexpr int DEP = 4;
  u32x4 A[DEP][4], W[DEP][5];
  auto ld = [&](int i) {
    int vo = lane * 16 + (i < nks ? 0 : 0x40000000);
#pragma unroll
    for (int m = 0; m < 4; m++) A[i % DEP][m] = __builtin_amdgcn_raw_buffer_load_b128(ra, vo + i * 1024, m * 54 * 1024, 0);
#pragma unroll
    for (int n = 0; n < 5; n++) W[i % DEP][n] = __builtin_amdgcn_raw_buffer_load_b128(rw, vo + i * 1024, n * 54 * 1024, 0);
  };
#pragma unroll
  for (int i = 0; i < DEP; i++) { ld(i); SB(); }
  const int tok = (mt0 + w) * 16 + (lane & 15), d0 = nt0 * 16 + (lane >> 4) * 4;
  u32x2 xr[5], gb[5];
#pragma unroll
  for (int n = 0; n < 5; n++) {
    xr[n] = *(const u32x2*)(ws + WS_X1 + ((size_t)tok * D + d0 + 16 * n) * 2);
    gb[n] = *(const u32x2*)(ws + WS_MOD + (5 * D + d0 + 16 * n) * 2);
  }
  f32x4 acc[5][4];
#pragma unroll
  for (int n = 0; n < 5; n++)
#pragma unroll
    for (int m = 0; m < 4; m++) acc[n][m] = (f32x4){0, 0, 0, 0};
#pragma unroll
  for (int i = 0; i < 14; i++) {
#pragma unroll
    for (int n = 0; n < 5; n++)
#pragma unroll
      for (int m = 0; m < 4; m++) acc[n][m] = mfma16(W[i % DEP][n], A[i % DEP][m], acc[n][m]);
    SB();
    if (i + DEP < 14) ld(i + DEP);
    SB();
    if (i == 0) { STAMP(3, 1); }
  }
  STAMP(3, 2);
#pragma unroll
  for (int m = 0; m < 4; m++) {
    if (m != w) {
      int slot = (w - m - 1) & 3;
#pragma unroll
      for (int n = 0; n < 5; n++) *(LDS f32x4*)(sm + ((m * 3 + slot) * 5 + n) * 1024 + lane * 16) = acc[n][m];
    }
  }
  bar();
#pragma unroll
  for (int n = 0; n < 5; n++) {
    f32x4 a = w == 0 ? acc[n][0] : w == 1 ? acc[n][1] : w == 2 ? acc[n][2] : acc[n][3];
#pragma unroll
    for (int sl = 0; sl < 3; sl++) a += *(LDS f32x4*)(sm + ((w * 3 + sl) * 5 + n) * 1024 + lane * 16);
    u32x2 o = {pk2(blo(xr[n].x) + blo(gb[n].x) * a[0], bhi(xr[n].x) + bhi(gb[n].x) * a[1]),
               pk2(blo(xr[n].y) + blo(gb[n].y) * a[2], bhi(xr[n].y) + bhi(gb[n].y) * a[3])};
    *(u32x2*)(out + (size_t)tok * D + d0 + 16 * n) = o;
  }
  STAMP(3, 3);
}

extern "C" void kernel_launch(void* const* d_in, const int* in_sizes, int n_in, void* d_out, int out_size, void* d_ws, size_t ws_size,
                              hipStream_t stream) {
  const u16* q = (const u16*)d_in[0];
  char* ws = (char*)d_ws;
  (void)hipFuncSetAttribute((const void*)k3, hipFuncAttributeMaxDynamicSharedMemorySize, 3 * G1_STAGE);
  k1<<<256, 256, 0, stream>>>((const u16*)d_in[1], (const u16*)d_in[2], (const u16*)d_in[3], (const u16*)d_in[4], (const u16*)d_in[5],
                              (const u16*)d_in[10], (const u16*)d_in[11], (const u16*)d_in[7], (const u16*)d_in[8], (const u16*)d_in[9], ws);
  k2<<<256, 640, 0, stream>>>(q, ws);
  k3<<<256, 256, 3 * G1_STAGE, stream>>>(ws);
  k4<<<256, 256, 0, stream>>>(ws, (u16*)d_out);
}
```

```cpp
#define STAMPS
#define PROBE 6
#include <hip/hip_runtime.h>
#include <stdint.h>

typedef unsigned short u16;
typedef unsigned int u32;
typedef unsigned long long u64;
typedef __attribute__((ext_vector_type(8))) __bf16 bf16x8;
typedef __attribute__((ext_vector_type(2))) __bf16 bf16x2;
typedef __attribute__((ext_vector_type(2))) float f32x2;
typedef __attribute__((ext_vector_type(4))) float f32x4;
typedef __attribute__((ext_vector_type(4))) u32 u32x4;
typedef __attribute__((ext_vector_type(2))) u32 u32x2;
typedef __attribute__((ext_vector_type(4))) short s16x4;
#define LDS __attribute__((address_space(3)))
#define DEV __device__ __forceinline__
#define SB() __builtin_amdgcn_sched_barrier(0)

constexpr int S = 2048, D = 640, F = 1728;
constexpr size_t WS_MOD = 0;
constexpr size_t WS_ZT  = 64 << 10;
constexpr size_t WS_X1  = WS_ZT + (size_t)S * D * 2;
constexpr size_t WS_H2  = WS_X1 + (size_t)S * D * 2;
constexpr size_t WS_W1  = WS_H2 + (size_t)S * D * 2;
constexpr size_t WS_W2  = WS_W1 + (size_t)2 * D * F * 2;
constexpr size_t WS_ACT = WS_W2 + (size_t)D * F * 2;
constexpr size_t WS_ACTL = WS_ACT + (size_t)128 * 48 * 1024;
constexpr size_t WS_FLAG = WS_ACT + (size_t)S * F * 2;
constexpr int FLAG_BYTES = (64 + 512) * 128;
constexpr size_t WS_END = WS_FLAG + FLAG_BYTES;
constexpr size_t WS_STAMP = 64 << 20;

#define AUX_IN 0
#define AUX_ST 0

struct Stamps { u32 t[16], c[16]; };
#define STAMP_DECL Stamps stT = {};
#define STAMP(i) do { SB(); stT.t[i] = (u32)__builtin_amdgcn_s_memrealtime(); stT.c[i] = (u32)__builtin_amdgcn_s_memtime(); SB(); } while (0)
#define STAMP_OUT(k) do { if ((threadIdx.x & 255) == 0) { u32* o = (u32*)(ws + WS_STAMP) + ((k) * 256 + blockIdx.x) * 32; _Pragma("unroll") for (int i_ = 0; i_ < 16; i_++) { o[i_] = stT.t[i_]; o[16 + i_] = stT.c[i_]; } } } while (0)

DEV float blo(u32 x) { return __uint_as_float(x << 16); }
DEV float bhi(u32 x) { return __uint_as_float(x & 0xffff0000u); }
DEV u32 pk2(float a, float b) { f32x2 v = {a, b}; return __builtin_bit_cast(u32, __builtin_convertvector(v, bf16x2)); }
DEV f32x4 mfma16(u32x4 a, u32x4 b, f32x4 c) {
  return __builtin_amdgcn_mfma_f32_16x16x32_bf16(__builtin_bit_cast(bf16x8, a), __builtin_bit_cast(bf16x8, b), c, 0, 0, 0);
}
DEV float silu_bf(u32 c) { float x = blo(c); return blo(pk2(x / (1.f + __expf(-x)), 0.f)); }
DEV __amdgpu_buffer_rsrc_t rsrc(const void* p, u32 bytes) { return __builtin_amdgcn_make_buffer_rsrc((void*)p, 0, bytes, 0x00020000); }
DEV float shr1(float x) { return __int_as_float(__builtin_amdgcn_update_dpp(0, __float_as_int(x), 0x138, 0xf, 0xf, true)); }
DEV float xsum(float x) {
  x += __int_as_float(__builtin_amdgcn_update_dpp(0, __float_as_int(x), 0x128, 0xf, 0xf, true));
  auto a = __builtin_amdgcn_permlane16_swap(__float_as_uint(x), __float_as_uint(x), false, false);
  x = __uint_as_float(a[0]) + __uint_as_float(a[1]);
  auto b = __builtin_amdgcn_permlane32_swap(__float_as_uint(x), __float_as_uint(x), false, false);
  return __uint_as_float(b[0]) + __uint_as_float(b[1]);
}
DEV u32x2 trread(LDS char* p) { return __builtin_bit_cast(u32x2, __builtin_amdgcn_ds_read_tr16_b64_v4i16((LDS s16x4*)p)); }
DEV void bar() { asm volatile("s_waitcnt lgkmcnt(0)" ::: "memory"); __builtin_amdgcn_s_barrier(); asm volatile("" ::: "memory"); }

constexpr int KC_COPY = 4160;
constexpr int UR_BYTES = 4608;
constexpr int K1_LDS = 4 * 2 * KC_COPY + 4 * UR_BYTES;

DEV int left_tile(int li) { int q = li / 3; return ((27 * (4 * q + 1 + li - 3 * q)) >> 2) + 6; }

DEV void retile_unit(u32x4 (&v)[4], LDS char* buf, char* dst, int fragstride, bool kperm, int lane) {
#pragma unroll
  for (int i = 0; i < 4; i++) {
    int c = lane + 64 * i, r = c >> 3, ch = c & 7;
    int h = ((r >> 1) & 1) | ((((r >> 2) ^ (r >> 3)) & 1) << 1);
    *(LDS u32x4*)(buf + r * 128 + ((ch ^ (h << 1)) << 4)) = v[i];
  }
  int g = lane >> 4, q = (lane >> 2) & 3, p = lane & 3;
  int r0 = kperm ? 4 * g + q : 8 * g + q, r1 = kperm ? r0 + 16 : r0 + 4;
  int h0 = ((r0 >> 1) & 1) | ((((r0 >> 2) ^ (r0 >> 3)) & 1) << 1);
  int h1 = ((r1 >> 1) & 1) | ((((r1 >> 2) ^ (r1 >> 3)) & 1) << 1);
#pragma unroll
  for (int t = 0; t < 4; t++) {
    u32x2 a = trread(buf + r0 * 128 + ((t ^ h0) << 5) + p * 8);
    u32x2 b = trread(buf + r1 * 128 + ((t ^ h1) << 5) + p * 8);
    u32x4 o = {a.x, a.y, b.x, b.y};
    __builtin_amdgcn_raw_buffer_store_b128(o, rsrc(dst, 4 * fragstride), lane * 16, t * fragstride, AUX_ST);
  }
}

#define PF_STEP 12
#define SIDE_DELAY 4
#define XRN 5
#define AUX_PF 0
#define AUX_G 0
#define AUX_W 0
__global__ __launch_bounds__(512, 2) void k1(const u16* __restrict__ kk, const u16* __restrict__ vv, const u16* __restrict__ shk,
                                             const u16* __restrict__ ssk, const u16* __restrict__ ssd, const u16* __restrict__ cc,
                                             const u16* __restrict__ wmod, const u16* __restrict__ wg, const u16* __restrict__ wu,
                                             const u16* __restrict__ wd, char* __restrict__ ws) {
  __shared__ __attribute__((aligned(16))) char smem[K1_LDS];
  LDS char* sm = (LDS char*)smem;
  const int tid = threadIdx.x, lane = tid & 63, w = __builtin_amdgcn_readfirstlane(tid >> 6);
  const int b = blockIdx.x, xcd = b & 7, bi = b >> 3;
  STAMP_DECL
  STAMP(0);
  auto rk = rsrc(kk, S * D * 2), rv = rsrc(vv, S * D * 2);
  LDS char* urb = sm + 4 * 2 * KC_COPY;
  const int cg = xcd * 20 + bi;
  if (tid < FLAG_BYTES / 256 / 16) *(u32x4*)(ws + WS_FLAG + (b * (FLAG_BYTES / 256 / 16) + tid) * 16) = (u32x4){0, 0, 0, 0};
  auto warm_kv = [&]() {
    u32 pf = 0;
    for (int i = 0; i < w; i++) __builtin_amdgcn_s_sleep(PF_STEP);
    const int pofs = (512 * w + 16 * bi + (lane & 15)) * 1280 + ((160 * xcd) & ~127) + ((lane >> 4) & 1) * 128;
    pf = *(const u32*)((const char*)(lane >= 32 ? vv : kk) + pofs);
    return pf;
  };
  if (w >= 4) {
    if (bi < 20) {
      const int w4 = w - 4;
      constexpr int P = 2;
      u32x2 gk[9], gv[9];
      auto ld = [&](int r) {
        int off = (61 * (4 * r + w4) - 3 + lane) * 1280 + cg * 8;
        gk[r] = __builtin_amdgcn_raw_buffer_load_b64(rk, off, 0, AUX_G);
        gv[r] = __builtin_amdgcn_raw_buffer_load_b64(rv, off, 0, AUX_G);
        SB();
      };
#pragma unroll
      for (int r = 0; r < P; r++) ld(r);
      STAMP(1);
      float cf[4][4];
      {
        const u32x4* sp = (const u32x4*)(shk + cg * 16);
        u32x4 s0 = sp[0], s1 = sp[1];
        cf[0][0] = blo(s0.x); cf[0][1] = bhi(s0.x); cf[0][2] = blo(s0.y); cf[0][3] = bhi(s0.y);
        cf[1][0] = blo(s0.z); cf[1][1] = bhi(s0.z); cf[1][2] = blo(s0.w); cf[1][3] = bhi(s0.w);
        cf[2][0] = blo(s1.x); cf[2][1] = bhi(s1.x); cf[2][2] = blo(s1.y); cf[2][3] = bhi(s1.y);
        cf[3][0] = blo(s1.z); cf[3][1] = bhi(s1.z); cf[3][2] = blo(s1.w); cf[3][3] = bhi(s1.w);
      }
#pragma unroll
      for (int r = 0; r < 9; r++) {
        int row = 61 * (4 * r + w4) - 3 + lane;
        float kf[4] = {blo(gk[r].x), bhi(gk[r].x), blo(gk[r].y), bhi(gk[r].y)};
        float vf[4] = {blo(gv[r].x), bhi(gv[r].x), blo(gv[r].y), bhi(gv[r].y)};
        float u[4];
#pragma unroll
        for (int c = 0; c < 4; c++) {
          float t = kf[c] * cf[c][3];
          t = shr1(t) + kf[c] * cf[c][2];
          t = shr1(t) + kf[c] * cf[c][1];
          t = shr1(t) + kf[c] * cf[c][0];
          u[c] = t * vf[c];
        }
        u32 p01 = pk2(u[0], u[1]), p23 = pk2(u[2], u[3]);
        if (lane >= 3 && row < S) {
          LDS u16* up = (LDS u16*)(urb + (2047 - row) * 2);
          up[0] = (u16)p01; up[UR_BYTES / 2] = (u16)(p01 >> 16);
          up[UR_BYTES] = (u16)p23; up[3 * UR_BYTES / 2] = (u16)(p23 >> 16);
        }
        STAMP(2 + r);
        bar();
        SB();
        if (r + P < 9) ld(r + P);
      }
      STAMP(15); STAMP_OUT(1);
      return;
    }
  } else if (bi < 20) {
    const int d = cg * 4 + w;
    auto rs = rsrc(ssk, S * D * 2);
    u32x4 kw[4];
#pragma unroll
    for (int i = 0; i < 4; i++) kw[i] = __builtin_amdgcn_raw_buffer_load_b128(rs, d * 4096 + (lane + 64 * i) * 16, 0, AUX_IN);
    SB();
    const u32 pf = warm_kv();
    SB();
    const u32 ddw = ((const u32*)ssd)[d >> 1];
    const float dd = (d & 1) ? bhi(ddw) : blo(ddw);
    LDS char* kc = sm + w * 2 * KC_COPY;
    LDS char* ur = urb + w * UR_BYTES;
    const u32x4 z4 = {0, 0, 0, 0};
    if (lane < 32) *(LDS u32x4*)(ur + 4096 + lane * 16) = z4;
#pragma unroll
    for (int i = 0; i < 4; i++) {
      u32 nx0 = i < 3 ? __builtin_amdgcn_readfirstlane(kw[i < 3 ? i + 1 : i].x) : 0u;
      u32 nx = __builtin_amdgcn_update_dpp(nx0, kw[i].x, 0x130, 0xf, 0xf, false);
      u32x4 o = {__builtin_amdgcn_alignbit(kw[i].y, kw[i].x, 16), __builtin_amdgcn_alignbit(kw[i].z, kw[i].y, 16),
                 __builtin_amdgcn_alignbit(kw[i].w, kw[i].z, 16), __builtin_amdgcn_alignbit(nx, kw[i].w, 16)};
      *(LDS u32x4*)(kc + 32 + (lane + 64 * i) * 16) = kw[i];
      *(LDS u32x4*)(kc + KC_COPY + 32 + (lane + 64 * i) * 16) = o;
    }
    if (lane == 0) {
      u32x4 e = {0, 0, 0, kw[0].x << 16};
      *(LDS u32x4*)(kc) = z4; *(LDS u32x4*)(kc + 16) = z4;
      *(LDS u32x4*)(kc + KC_COPY) = z4; *(LDS u32x4*)(kc + KC_COPY + 16) = e;
    }
    const int a = lane & 15, g = lane >> 4;
    LDS const u32* ap = (LDS const u32*)(kc + ((a & 1) ? 0 : KC_COPY)) + 4 * g + ((a + 1) >> 1);
    LDS const u32* ab[4];
#pragma unroll
    for (int i = 0; i < 4; i++) { ab[i] = ap + 256 * i; asm volatile("" : "+v"(ab[i])); }
    auto rdA = [&](int m) { LDS const u32* p = ab[m >> 4] + 16 * (m & 15); return (u32x4){p[0], p[1], p[2], p[3]}; };
    constexpr int XR = XRN;
    u32x4 A[8 * XR];
#pragma unroll
    for (int m = 0; m < 8 * XR; m++) A[m] = rdA(m);
    STAMP(1);
    f32x4 acc[8];
#pragma unroll
    for (int n = 0; n < 8; n++) acc[n] = (f32x4){0, 0, 0, 0};
    LDS char* bp = ur + (2032 - 16 * a + 8 * g) * 2;
    auto rz = rsrc(ws + WS_ZT + (size_t)d * 4096, 4096);
    u32x4 As[2][8 - XR], B[2];
    u32x2 uu;
    auto zout = [&](int y) {
      u32x2 o = {pk2(acc[y][0] + bhi(uu.y) * dd, acc[y][1] + blo(uu.y) * dd), pk2(acc[y][2] + bhi(uu.x) * dd, acc[y][3] + blo(uu.x) * dd)};
      __builtin_amdgcn_raw_buffer_store_b64(o, rz, (16 * a + 4 * g) * 2, 512 * y, AUX_ST);
    };
#pragma unroll
    for (int x = XR; x < 8; x++) As[1][x - XR] = rdA(8 * x + 7);
    bar();
    B[1] = *(LDS u32x4*)(bp + 32 * 7 * 2);
#pragma unroll
    for (int y = 0; y < 8; y++) {
#pragma unroll
      for (int rho = 7; rho >= 0; rho--) {
        const int yn = rho ? y : y + 1, rn = (rho + 7) & 7;
        if (rho == 3) { __builtin_amdgcn_s_barrier(); asm volatile("" ::: "memory"); }
        if (yn < 8) B[rn & 1] = *(LDS u32x4*)(bp + (32 * rn - 256 * yn) * 2);
#pragma unroll
        for (int x = XR; x < 8; x++)
          if (x < 8 - yn) As[rn & 1][x - XR] = rdA(8 * x + rn);
        SB();
#pragma unroll
        for (int x = 0; x < 8 - y; x++) acc[x + y] = mfma16(x < XR ? A[x < XR ? 8 * x + rho : 0] : As[rho & 1][x < XR ? 0 : x - XR], B[rho & 1], acc[x + y]);
        SB();
        if (rho == 6 && y > 0) zout(y - 1);
        if (rho == 2) uu = *(LDS u32x2*)(ur + (2044 - 256 * y - 16 * a - 4 * g) * 2);
        SB();
      }
      STAMP(2 + y);
    }
    zout(7);
    asm volatile("" ::"v"(pf));
    STAMP(15); STAMP_OUT(0);
    return;
  }
  {
    const int sid = (bi - 20) * 8 + xcd;
    u32 pf = 0;
    if (w < 4) pf = warm_kv();
    for (int i = w < 4 ? w : 0; i < SIDE_DELAY; i++) __builtin_amdgcn_s_sleep(PF_STEP);
    int u0, nu;
    if (sid < 60) {
      u0 = sid * 9; nu = 9;
      auto rw = rsrc(wmod, D * 6 * D * 2);
      const int ch = tid & 7, rg = tid >> 3;
      u32x4 wv[10];
      u16 c0 = cc[tid], c1 = cc[512 + (tid & 127)];
      SB();
#pragma unroll
      for (int i = 0; i < 10; i++) wv[i] = __builtin_amdgcn_raw_buffer_load_b128(rw, (rg + 64 * i) * 7680 + sid * 128 + ch * 16, 0, AUX_IN);
      LDS float* sil = (LDS float*)(sm + 32768);
      SB();
      sil[tid] = silu_bf(c0); sil[512 + (tid & 127)] = silu_bf(c1);
      bar();
      float ac[8] = {0, 0, 0, 0, 0, 0, 0, 0};
#pragma unroll
      for (int i = 0; i < 10; i++) {
        float s = sil[rg + 64 * i];
        ac[0] += s * blo(wv[i].x); ac[1] += s * bhi(wv[i].x); ac[2] += s * blo(wv[i].y); ac[3] += s * bhi(wv[i].y);
        ac[4] += s * blo(wv[i].z); ac[5] += s * bhi(wv[i].z); ac[6] += s * blo(wv[i].w); ac[7] += s * bhi(wv[i].w);
      }
#pragma unroll
      for (int j = 0; j < 8; j++) ac[j] = xsum(ac[j]);
      LDS float* red = (LDS float*)(sm + 36864);
      if (lane < 8) {
        *(LDS f32x4*)(red + w * 64 + lane * 8) = (f32x4){ac[0], ac[1], ac[2], ac[3]};
        *(LDS f32x4*)(red + w * 64 + lane * 8 + 4) = (f32x4){ac[4], ac[5], ac[6], ac[7]};
      }
      bar();
      if (tid < 64) {
        float t = 0;
#pragma unroll
        for (int i = 0; i < 8; i++) t += red[i * 64 + tid];
        ((u16*)(ws + WS_MOD))[sid * 64 + tid] = (u16)pk2(t, 0.f);
      }
    } else {
      u0 = 540 + (sid - 60) * 30; nu = 30;
    }
    STAMP(1);
    LDS char* buf = sm + w * 4096;
    u32x4 v[4][4];
    char* dst[4]; int fstr[4];
#pragma unroll
    for (int jj = 0; jj < 4; jj++) {
      int u = u0 + w + 8 * jj;
      if (w + 8 * jj < nu) {
        const char* src; int pitch, hi16 = 16;
        if (u < 1080) {
          int mat = u >= 540, uu = u - 540 * mat, ks = uu / 27, fq = uu - 27 * ks;
          src = (const char*)(mat ? wu : wg) + ks * 32 * 3456 + fq * 128; pitch = 3456;
          dst[jj] = ws + WS_W1 + ((size_t)(mat * 108 + fq * 4) * 20 + ks) * 1024; fstr[jj] = 20 * 1024;
        } else {
          int uu = u - 1080, ks = uu / 10, nq = uu - 10 * ks, fa;
          if (ks < 48) fa = ((27 * (ks & 15)) >> 2) + 2 * (ks >> 4);
          else { int l0 = 2 * (ks - 48), l1 = l0 + 1; fa = left_tile(l0); hi16 = 16 * (left_tile(l1) - fa); }
          src = (const char*)wd + fa * 16 * 1280 + nq * 128; pitch = 1280;
          dst[jj] = ws + WS_W2 + ((size_t)(nq * 4) * 54 + ks) * 1024; fstr[jj] = 54 * 1024;
        }
        auto rr = rsrc(src, (hi16 + 16) * pitch);
#pragma unroll
        for (int i = 0; i < 4; i++) v[jj][i] = __builtin_amdgcn_raw_buffer_load_b128(rr, (lane >> 3) * pitch + (lane & 7) * 16, (8 * (i & 1) + (i >> 1) * hi16) * pitch, AUX_W);
      }
    }
#pragma unroll
    for (int jj = 0; jj < 4; jj++)
      if (w + 8 * jj < nu) retile_unit(v[jj], buf, dst[jj], fstr[jj], fstr[jj] != 20 * 1024, lane);
    asm volatile("" ::"v"(pf));
  }
  STAMP(15); if (w < 4) STAMP_OUT(0);
}

constexpr int RING = 7 * 20 * 1024, KF_ZB = RING, KF_RED = KF_ZB + 10240, KF_DONE = KF_RED + 1280, KF_LDS = KF_DONE + 16;
constexpr int SC1 = 16;
typedef __attribute__((address_space(1))) u32 gu32;
DEV void set_flag(char* p) { __hip_atomic_store((gu32*)p, 1u, __ATOMIC_RELAXED, __HIP_MEMORY_SCOPE_AGENT); }
DEV u32 peek_flags(char* p, int n, int lane) { return __hip_atomic_load((gu32*)p + (lane & (n - 1)), __ATOMIC_RELAXED, __HIP_MEMORY_SCOPE_AGENT); }
DEV void wait_flags(char* p, int n, int lane) {
  gu32* q = (gu32*)p + (lane & (n - 1));
#pragma unroll 1
  for (int spin = 0; spin < (1 << 20); spin++) {
    if (__all(__hip_atomic_load(q, __ATOMIC_RELAXED, __HIP_MEMORY_SCOPE_AGENT) != 0)) break;
    __builtin_amdgcn_s_sleep(1);
  }
  asm volatile("" ::: "memory");
}

DEV float dot2(u32 a, u32 b, float c) { return __builtin_amdgcn_fdot2_f32_bf16(__builtin_bit_cast(bf16x2, a), __builtin_bit_cast(bf16x2, b), c, false); }
DEV void row_stats(u32x4 v, LDS char* dst, int lane) {
  const u32 one = 0x3f803f80u;
  float s = dot2(v.w, one, dot2(v.z, one, dot2(v.y, one, dot2(v.x, one, 0.f))));
  float q = dot2(v.w, v.w, dot2(v.z, v.z, dot2(v.y, v.y, dot2(v.x, v.x, 0.f))));
  auto a = __builtin_amdgcn_permlane32_swap(__float_as_uint(s), __float_as_uint(q), false, false);
  float x = __uint_as_float(a[0]) + __uint_as_float(a[1]);
  auto b = __builtin_amdgcn_permlane16_swap(__float_as_uint(x), __float_as_uint(x), false, false);
  x = __uint_as_float(b[0]) + __uint_as_float(b[1]);
  x += __int_as_float(__builtin_amdgcn_update_dpp(0, __float_as_int(x), 0x128, 0xf, 0xf, true));
  if ((lane & 24) == 0) *(LDS float*)(dst + (lane & 7) * 8 + (lane >> 5) * 4) = x;
}
DEV f32x2 mean_rstd(LDS char* red, int t) {
  f32x2 a = {0, 0};
#pragma unroll
  for (int i = 0; i < 10; i++) a += *(LDS f32x2*)(red + i * 64 + t * 8);
  float mean = a.x * (1.f / D), rstd = __builtin_amdgcn_rsqf(fmaxf(a.y * (1.f / D) - mean * mean, 0.f) + 1e-5f);
  return (f32x2){rstd, -mean * rstd};
}
DEV void unpack8(u32x4 v, f32x2 (&f)[4]) {
  f[0] = (f32x2){blo(v.x), bhi(v.x)}; f[1] = (f32x2){blo(v.y), bhi(v.y)}; f[2] = (f32x2){blo(v.z), bhi(v.z)}; f[3] = (f32x2){blo(v.w), bhi(v.w)};
}
DEV u32x4 pack8(f32x2 (&f)[4]) { return (u32x4){pk2(f[0].x, f[0].y), pk2(f[1].x, f[1].y), pk2(f[2].x, f[2].y), pk2(f[3].x, f[3].y)}; }

template <int NI>
DEV void ln_part(const u16* __restrict__ qq, char* __restrict__ ws, LDS char* sm, int s0, int w, int lane, int pfo
  , Stamps& stT
) {
  const int t = lane & 7, s = s0 + t;
  u32x4 zr[NI], qv[NI], msa[NI], mca[NI], mga[NI], msb[NI], mcb[NI];
  int wi[NI], c[NI];
#pragma unroll
  for (int i = 0; i < NI; i++) {
    wi[i] = i == 0 ? w : w + 2;
    c[i] = wi[i] * 8 + (lane >> 3);
    qv[i] = *(const u32x4*)(qq + s * D + 8 * c[i]);
    const u32x4* mp = (const u32x4*)(ws + WS_MOD) + c[i];
    msa[i] = mp[0]; mca[i] = mp[80]; mga[i] = mp[160]; msb[i] = mp[240]; mcb[i] = mp[320];
  }
  SB();
#pragma unroll
  for (int i = 0; i < NI; i++) zr[i] = *(const u32x4*)(ws + WS_ZT + (size_t)(wi[i] * 64 + lane) * 4096 + s0 * 2);
  SB();
  f32x2 x[NI][4], sa[NI][4], ca[NI][4], ga[NI][4];
#pragma unroll
  for (int i = 0; i < NI; i++) {
    row_stats(qv[i], sm + KF_RED + wi[i] * 64, lane);
    unpack8(qv[i], x[i]); unpack8(msa[i], sa[i]); unpack8(mca[i], ca[i]); unpack8(mga[i], ga[i]);
  }
  SB();
#pragma unroll
  for (int i = 0; i < NI; i++) *(LDS u32x4*)(sm + KF_ZB + wi[i] * 1024 + lane * 16) = zr[i];
  STAMP(11);
  bar();
  u32 pf = 0;
  pf = __builtin_amdgcn_raw_buffer_load_b32(rsrc(ws + WS_W1, 2 * D * F * 2), pfo, 0, 0);
  STAMP(12);
  f32x2 mr = mean_rstd(sm + KF_RED, t);
  auto rx = rsrc(ws + WS_X1, S * D * 2), rh = rsrc(ws + WS_H2, S * D * 2);
  u32x4 x1[NI];
#pragma unroll
  for (int i = 0; i < NI; i++) {
    f32x2 z[4], p[4];
    int i16 = lane & 15, G = lane >> 4, q = i16 >> 2, pp = i16 & 3;
    LDS char* a = sm + KF_ZB + wi[i] * 1024 + (16 * G + 8 * (pp >> 1) + q) * 16 + (pp & 1) * 8;
    u32x2 z0 = trread(a), z1 = trread(a + 64);
    unpack8((u32x4){z0.x, z0.y, z1.x, z1.y}, z);
#pragma unroll
    for (int j = 0; j < 4; j++) { f32x2 n = x[i][j] * mr.x + mr.y, h = n * ca[i][j] + (n + sa[i][j]); p[j] = ga[i][j] * (h * z[j]) + x[i][j]; }
    x1[i] = pack8(p);
    row_stats(x1[i], sm + KF_RED + 640 + wi[i] * 64, lane);
    __builtin_amdgcn_raw_buffer_store_b128(x1[i], rx, (s * D + 8 * c[i]) * 2, 0, SC1);
    unpack8(x1[i], x[i]); unpack8(msb[i], sa[i]); unpack8(mcb[i], ca[i]);
  }
  bar();
  STAMP(13);
  mr = mean_rstd(sm + KF_RED + 640, t);
#pragma unroll
  for (int i = 0; i < NI; i++) {
    f32x2 o[4];
#pragma unroll
    for (int j = 0; j < 4; j++) { f32x2 n = x[i][j] * mr.x + mr.y; o[j] = n * ca[i][j] + (n + sa[i][j]); }
    __builtin_amdgcn_raw_buffer_store_b128(pack8(o), rh, (((s >> 4) * 20 + (c[i] >> 2)) * 64 + (c[i] & 3) * 16 + (s & 15)) * 16, 0, SC1);
  }
  STAMP(14);
  asm volatile("s_waitcnt vmcnt(0)" ::"v"(pf) : "memory");
}

#define PF2K 4
#define LASTBAR 9
#define HD 8
__global__ __launch_bounds__(512, 2) void kf(const u16* __restrict__ qq, char* __restrict__ ws, u16* __restrict__ out) {
  extern __shared__ __attribute__((aligned(16))) char smem[];
  LDS char* sm = (LDS char*)smem;
  const int tid = threadIdx.x, lane = tid & 63, w = __builtin_amdgcn_readfirstlane(tid >> 6);
  const int xcd = blockIdx.x & 7, bi = blockIdx.x >> 3;
  const int M = 8 * (xcd >> 2) + (bi & 7), fs = 4 * (xcd & 3) + (bi >> 3), ft0 = (27 * fs) >> 2;
  const int hm = fs >> 3, nt0 = (fs & 7) * 5, mg = M * 8 + hm * 4;
  char* flagH = ws + WS_FLAG + M * 4 * 128;
  char* flagA = ws + WS_FLAG + 64 * 128 + M * 32 * 128;
  STAMP_DECL
  STAMP(0);
  if (tid == 0) *(LDS u32*)(sm + KF_DONE) = 0;
#define STARG , stT
  const int pl = bi * 270 + tid, pfo = tid < 270 ? ((pl >= 4320 ? 108 * 160 - 4320 : 0) + (xcd & 3) * 4320 + pl) * 128 : -1;
  if (w >= 6) ln_part<2>(qq, ws, sm, M * 128 + fs * 8, w, lane, pfo STARG); else ln_part<1>(qq, ws, sm, M * 128 + fs * 8, w, lane, pfo STARG);
  __builtin_amdgcn_s_barrier();
  if (tid == 0) set_flag(flagH + (fs >> 2) * 128 + (fs & 3) * 4);
  STAMP(1);

  auto ra = rsrc(ws + WS_ACT, S * F * 2);
  auto rw2 = rsrc(ws + WS_W2 + (size_t)nt0 * 54 * 1024, 5 * 54 * 1024);
  f32x4 oc[5][4];
  auto dmm = [&](u32x4 (&A)[4], u32x4 (&W)[5]) {
#pragma unroll
    for (int n = 0; n < 5; n++)
#pragma unroll
      for (int m = 0; m < 4; m++) oc[n][m] = mfma16(W[n], A[m], oc[n][m]);
  };
  auto ldW2 = [&](u32x4 (&W)[5], int kq) {
#pragma unroll
    for (int n = 0; n < 5; n++) W[n] = __builtin_amdgcn_raw_buffer_load_b128(rw2, lane * 16, (n * 54 + kq) * 1024, 0);
  };
  auto ldA = [&](u32x4 (&A)[4], int kq) {
#pragma unroll
    for (int m = 0; m < 4; m++) A[m] = __builtin_amdgcn_raw_buffer_load_b128(ra, lane * 16, ((mg + m) * 48 + kq) * 1024, SC1);
  };

  if (w >= 4) {
    const int j = w - 4;
    auto rr = rsrc(ws + WS_W1 + (size_t)ft0 * 20 * 1024, 2 * D * F * 2 - ft0 * 20 * 1024);
    const int v1 = j * 1024 + lane * 16, v2 = ((j & 1) * 108 * 20 + (j >> 1) * 5) * 1024 + lane * 16;
    LDS char* l1 = sm + j * 6 * 1024;
    LDS char* l2 = sm + ((j >> 1) * 10 + (j & 1)) * 1024;
    auto issue = [&](int g) {
      if (g < 5) {
#pragma unroll
        for (int e = 0; e < 6; e++)
          __builtin_amdgcn_raw_ptr_buffer_load_lds(rr, (LDS void*)(l1 + (24 * g + e) * 1024), 16, v1, (((e & 1) * 108 + (e >> 1)) * 20 + 4 * g) * 1024, 0, 0);
      } else {
#pragma unroll
        for (int e = 0; e < 5; e++)
          __builtin_amdgcn_raw_ptr_buffer_load_lds(rr, (LDS void*)(l2 + ((20 * g + 20) % 140 + 2 * e) * 1024), 16, v2, ((3 + ((g - 5) >> 1)) * 20 + ((g - 5) & 1) * 10 + e) * 1024, 0, 0);
      }
    };
#define WV(n) asm volatile("s_waitcnt vmcnt(" #n ")" ::: "memory")
#define BR() __builtin_amdgcn_s_barrier()
    issue(0); issue(1); issue(2);
    STAMP(2);
    WV(12); BR();
    STAMP(3);
    issue(3); issue(4); issue(5);
    WV(23); BR();
    WV(17); BR(); issue(6); issue(7);
    WV(21); BR(); issue(8);
    WV(20); BR(); issue(9);
    WV(20); BR(); issue(10);
    {
      auto r2 = rsrc(ws + WS_W2 + (size_t)(xcd & 1) * 20 * 54 * 1024, 20 * 54 * 1024);
      __builtin_amdgcn_raw_ptr_buffer_load_lds(r2, (LDS void*)(sm + KF_ZB), 4, (bi * 270 + j * 68 + lane) * 128, 0, 0, 0);
      __builtin_amdgcn_raw_ptr_buffer_load_lds(r2, (LDS void*)(sm + KF_ZB), 4, lane < 4 ? (bi * 270 + j * 68 + 64 + lane) * 128 : -1, 0, 0, 0);
    }
    WV(22); BR(); issue(11);
    WV(22); BR(); issue(12);
    WV(20); BR();
    __builtin_amdgcn_s_waitcnt(0x0f70);
    wait_flags(flagA + ((hm * 4 + 0) * 4 + j) * 128, 8, lane);
    BR();
    STAMP(4);
#pragma unroll
    for (int n = 0; n < 5; n++)
#pragma unroll
      for (int m = 0; m < 4; m++) oc[n][m] = (f32x4){0, 0, 0, 0};
    u32x4 A[4][4], W[4][5];
    char* f1 = flagA + ((hm * 4 + 1) * 4 + j) * 128;
#pragma unroll
    for (int i = 0; i < 3; i++) { ldA(A[i], 4 * j + i); ldW2(W[i], 4 * j + i); SB(); }
    u32 early = peek_flags(f1, 8, lane);
    SB();
    ldA(A[3], 4 * j + 3); ldW2(W[3], 4 * j + 3); SB();
    STAMP(5);
    dmm(A[0], W[0]); SB();
    if (!__all(early != 0)) wait_flags(f1, 8, lane);
    STAMP(6);
#pragma unroll
    for (int i = 0; i < 4; i++) { if (i) dmm(A[i], W[i]); SB(); ldA(A[i], 16 + 4 * j + i); ldW2(W[i], 16 + 4 * j + i); SB(); }
#pragma unroll
    for (int i = 0; i < 4; i++) { dmm(A[i], W[i]); SB(); }
    STAMP(7);
    while (*(volatile LDS u32*)(sm + KF_DONE) < 4) __builtin_amdgcn_s_sleep(1);
#pragma unroll
    for (int n = 0; n < 5; n++)
#pragma unroll
      for (int m = 0; m < 4; m++) *(LDS f32x4*)(sm + (j * 20 + n * 4 + m) * 1024 + lane * 16) = oc[n][m];
    bar();
    STAMP(8);
    __builtin_amdgcn_s_barrier();
    STAMP(15); STAMP_OUT(3);
    return;
  }
  __builtin_amdgcn_s_setprio(2);
  {
    u32x4 H[20][2], W[8];
    f32x4 acc[3][2][2];
    auto rh = rsrc(ws + WS_H2 + (size_t)(M * 8 + 2 * w) * 20 * 1024, 40 * 1024);
    auto ldH = [&](int ks) {
      H[ks][0] = __builtin_amdgcn_raw_buffer_load_b128(rh, lane * 16, ks * 1024, SC1);
      H[ks][1] = __builtin_amdgcn_raw_buffer_load_b128(rh, lane * 16, (20 + ks) * 1024, SC1);
    };
    wait_flags(flagH + w * 128, 4, lane);
    STAMP(2);
#pragma unroll
    for (int ks = 0; ks < HD; ks++) ldH(ks);
    SB();
    LDS char* base = sm + lane * 16;
    auto rdW = [&](int i) {
      if (i < 120 ? i % 24 == 0 : i % 20 == 0 && i / 20 - 1 <= LASTBAR) { __builtin_amdgcn_s_barrier(); asm volatile("" ::: "memory"); }
      W[i & 7] = *(LDS u32x4*)(base + (i % 140) * 1024);
    };
    const f32x4 z4 = {0, 0, 0, 0};
#pragma unroll
    for (int i = 0; i < 6; i++) rdW(i);
    STAMP(3);
#pragma unroll
    for (int i = 0; i < 120; i++) {
      const int ks = i / 6, t = (i % 6) >> 1, mat = i & 1;
      rdW(i + 6);
      if (i % 6 == 0 && ks + HD < 20) ldH(ks + HD);
      SB();
      acc[t][mat][0] = mfma16(W[i & 7], H[ks][0], ks ? acc[t][mat][0] : z4);
      acc[t][mat][1] = mfma16(W[i & 7], H[ks][1], ks ? acc[t][mat][1] : z4);
      SB();
    }
    STAMP(4);
    const bool seven = (fs & 3) != 0;
    u32 pk[2][4];
    auto relu = [](float x) { return __int_as_float(max(__float_as_int(x), 0)); };
    auto quarter = [&](int sl, int q, int odd) {
      const int m = q >> 1, h = 2 * (q & 1);
      pk[m][2 * odd + (q & 1)] = pk2(relu(acc[sl][0][m][h]) * acc[sl][1][m][h], relu(acc[sl][0][m][h + 1]) * acc[sl][1][m][h + 1]);
    };
    auto store_pk = [&](int p, int m) {
      __builtin_amdgcn_raw_buffer_store_b128((u32x4){pk[m][0], pk[m][1], pk[m][2], pk[m][3]}, ra, lane * 16, ((M * 8 + 2 * w + m) * 48 + 16 * p + fs) * 1024, SC1);
    };
    const int fw = (fs & 3) * 2 + (w & 1);
    char* myflag = flagA + ((w >> 1) * 16 + (fs >> 2)) * 128 + fw * 4;
    auto publish = [&](char* f) {
      asm volatile("s_waitcnt vmcnt(0)" ::: "memory");
      if (lane == 0) set_flag(f);
    };
    auto tile = [&](int t, int sl, auto&& hook) {
#pragma unroll
      for (int jj = 0; jj < 40; jj++) {
        const int i = 120 + 40 * (t - 3) + jj, ks = jj >> 1, mat = jj & 1;
        if (i + 6 < 280) rdW(i + 6);
        SB();
        acc[sl][mat][0] = mfma16(W[i & 7], H[ks][0], ks ? acc[sl][mat][0] : z4);
        acc[sl][mat][1] = mfma16(W[i & 7], H[ks][1], ks ? acc[sl][mat][1] : z4);
        SB();
        hook(jj);
        SB();
      }
    };
    auto odd_hook = [&](int jj, int sl, int p) {
      if (jj >= 2 && jj <= 8 && !(jj & 1)) quarter(sl, (jj - 2) >> 1, 1);
      if (jj == 5) store_pk(p, 0);
      if (jj == 9) store_pk(p, 1);
      if (jj == 38) publish(myflag + p * 512);
    };
    auto even_hook = [&](int jj, int sl, int j0) { if (jj >= j0 && jj < j0 + 8 && !(jj & 1)) quarter(sl, (jj - j0) >> 1, 0); };
#pragma unroll
    for (int q = 0; q < 4; q++) quarter(0, q, 0);
    tile(3, 0, [&](int jj) { odd_hook(jj, 1, 0); even_hook(jj, 2, 12); });
    tile(4, 1, [&](int jj) { odd_hook(jj, 0, 1); });
    tile(5, 0, [&](int jj) { even_hook(jj, 1, 2); });
    char* lastflag;
    if (seven) {
      tile(6, 1, [&](int jj) { odd_hook(jj, 0, 2); });
      const int li = 3 * (fs >> 2) + (fs & 3) - 1;
#pragma unroll
      for (int q = 0; q < 4; q++) quarter(1, q, 0);
      __builtin_amdgcn_raw_buffer_store_b128((u32x4){pk[0][0], pk[0][1], pk[1][0], pk[1][1]}, ra, lane * 16, (int)(WS_ACTL - WS_ACT) + ((M * 4 + w) * 12 + li) * 1024, SC1);
      lastflag = flagA + (((w >> 1) * 4 + 3) * 4 + ((li >> 1) & 3)) * 128 + ((li >> 3) * 4 + (li & 1) * 2 + (w & 1)) * 4;
    } else {
#pragma unroll
      for (int q = 0; q < 4; q++) quarter(0, q, 1);
      store_pk(2, 0); store_pk(2, 1);
      lastflag = myflag + 2 * 512;
    }
    STAMP(5);
    if (lane == 0) __hip_atomic_fetch_add((LDS u32*)(sm + KF_DONE), 1u, __ATOMIC_RELAXED, __HIP_MEMORY_SCOPE_WORKGROUP);
    publish(lastflag);
  }
  STAMP(6);
  {
    u32x4 A[4][4], W[4][5], WL[2][5], L[2][4];
    char* f2 = flagA + ((hm * 4 + 2) * 4 + w) * 128;
    char* f3 = flagA + ((hm * 4 + 3) * 4 + w) * 128;
    wait_flags(f2, 8, lane);
#pragma unroll
    for (int i = 0; i < 4; i++) { ldA(A[i], 32 + 4 * w + i); ldW2(W[i], 32 + 4 * w + i); SB(); }
    STAMP(7);
#pragma unroll
    for (int n = 0; n < 5; n++)
#pragma unroll
      for (int m = 0; m < 4; m++) oc[n][m] = (f32x4){0, 0, 0, 0};
    dmm(A[0], W[0]); SB();
    u32 early = peek_flags(f3, w < 2 ? 8 : 4, lane); SB();
    dmm(A[1], W[1]); SB();
    ldW2(WL[0], 48 + w); ldW2(WL[1], w < 2 ? 52 + w : 0x10000); SB();
    dmm(A[2], W[2]); dmm(A[3], W[3]); SB();
    const int tok = (mg + w) * 16 + (lane & 15), d0 = nt0 * 16 + (lane >> 4) * 4;
    u32x2 xr[5], gb[5];
    auto rx = rsrc(ws + WS_X1, S * D * 2);
    STAMP(8);
    if (!__all(early != 0)) wait_flags(f3, w < 2 ? 8 : 4, lane);
    STAMP(9);
#pragma unroll
    for (int i = 0; i < 2; i++)
#pragma unroll
      for (int e = 0; e < 4; e++)
        L[i][e] = __builtin_amdgcn_raw_buffer_load_b128(ra, lane * 16, i && w >= 2 ? 0x40000000 : (int)(WS_ACTL - WS_ACT) + ((M * 4 + hm * 2 + (e >> 1)) * 12 + 2 * (w + 4 * i) + (e & 1)) * 1024, SC1);
#pragma unroll
    for (int n = 0; n < 5; n++) {
      xr[n] = __builtin_amdgcn_raw_buffer_load_b64(rx, (tok * D + d0 + 16 * n) * 2, 0, SC1);
      gb[n] = *(const u32x2*)(ws + WS_MOD + (5 * D + d0 + 16 * n) * 2);
    }
    SB();
    __builtin_amdgcn_s_barrier();
    asm volatile("" ::: "memory");
#pragma unroll
    for (int n = 0; n < 5; n++)
#pragma unroll
      for (int m = 0; m < 4; m++) oc[n][m] += *(LDS f32x4*)(sm + (w * 20 + n * 4 + m) * 1024 + lane * 16);
    SB();
#pragma unroll
    for (int i = 0; i < 2; i++) {
      u32x4 AL[4] = {{L[i][0].x, L[i][0].y, L[i][1].x, L[i][1].y}, {L[i][0].z, L[i][0].w, L[i][1].z, L[i][1].w},
                     {L[i][2].x, L[i][2].y, L[i][3].x, L[i][3].y}, {L[i][2].z, L[i][2].w, L[i][3].z, L[i][3].w}};
      dmm(AL, WL[i]);
    }
    STAMP(10);
    LDS char* rb = sm + 80 * 1024 + lane * 16;
#pragma unroll
    for (int m = 0; m < 4; m++) {
      if (m != w) {
        int slot = (w - m - 1) & 3;
#pragma unroll
        for (int n = 0; n < 5; n++) *(LDS f32x4*)(rb + ((m * 3 + slot) * 5 + n) * 1024) = oc[n][m];
      }
    }
    bar();
    auto fin = [&](int m) {
#pragma unroll
      for (int n = 0; n < 5; n++) {
        f32x4 a = oc[n][m];
#pragma unroll
        for (int sl = 0; sl < 3; sl++) a += *(LDS f32x4*)(rb + ((m * 3 + sl) * 5 + n) * 1024);
        u32x2 o = {pk2(blo(xr[n].x) + blo(gb[n].x) * a[0], bhi(xr[n].x) + bhi(gb[n].x) * a[1]),
                   pk2(blo(xr[n].y) + blo(gb[n].y) * a[2], bhi(xr[n].y) + bhi(gb[n].y) * a[3])};
        *(u32x2*)(out + (size_t)tok * D + d0 + 16 * n) = o;
      }
    };
    if (w == 0) fin(0); else if (w == 1) fin(1); else if (w == 2) fin(2); else fin(3);
  }
  STAMP(15); STAMP_OUT(2);
}

__global__ void k_spin(char* ws) {
  __shared__ u32 mn[256], mx[256];
  const u32* h = (const u32*)(ws + WS_STAMP);
  int b = threadIdx.x;
  auto get = [&](int k, int i) { return h[(k * 256 + b) * 32 + i]; };
  u32 base = __builtin_amdgcn_readfirstlane(h[0]) - 100000u;
  auto red = [&](u32 v, int mode) {
    v -= base; mn[b] = v; __syncthreads(); u32 r = mode == 0 ? ~0u : 0u;
    for (int i = 0; i < 256; i++) { u32 x = mn[i]; r = mode == 0 ? min(r, x) : mode == 1 ? max(r, x) : r + x; }
    __syncthreads(); return mode == 2 ? r / 256 : r; };
  u32 X = 0;
  if (PROBE == 1) X = red(get(0, 15), 1) - red(get(0, 0), 0);
  if (PROBE == 2) X = red(get(2, 0), 0) - red(get(0, 15), 1);
  if (PROBE == 3) X = red(get(2, 3), 2) - red(get(2, 0), 0);
  if (PROBE == 4) X = red(get(2, 5), 2) - red(get(2, 3), 2);
  if (PROBE == 5) X = red(get(2, 15), 1) - red(get(2, 5), 2);
  if (PROBE == 7) X = red(get(1, b < 160 ? 2 : 0), 1) - red(get(0, 0), 0);
  if (PROBE == 8) X = (h[(2 * 256 + 9) * 32 + 16 + 5] - h[(2 * 256 + 9) * 32 + 16 + 3]) / 20;
  if (X > 10000) X = 10000;
  u64 t0 = __builtin_amdgcn_s_memrealtime();
  while (__builtin_amdgcn_s_memrealtime() - t0 < (u64)X * 50) __builtin_amdgcn_s_sleep(2);
}

extern "C" void kernel_launch(void* const* d_in, const int* in_sizes, int n_in, void* d_out, int out_size, void* d_ws, size_t ws_size,
                              hipStream_t stream) {
  const u16* q = (const u16*)d_in[0];
  char* ws = (char*)d_ws;
  (void)hipFuncSetAttribute((const void*)kf, hipFuncAttributeMaxDynamicSharedMemorySize, KF_LDS);
  k1<<<256, 512, 0, stream>>>((const u16*)d_in[1], (const u16*)d_in[2], (const u16*)d_in[3], (const u16*)d_in[4], (const u16*)d_in[5],
                              (const u16*)d_in[10], (const u16*)d_in[11], (const u16*)d_in[7], (const u16*)d_in[8], (const u16*)d_in[9], ws);
  kf<<<256, 512, KF_LDS, stream>>>(q, ws, (u16*)d_out);
  k_spin<<<1, 256, 0, stream>>>(ws);
}
```

```cpp
#include <hip/hip_runtime.h>
#include <stdint.h>

typedef unsigned short u16;
typedef unsigned int u32;
typedef unsigned long long u64;
typedef __attribute__((ext_vector_type(8))) __bf16 bf16x8;
typedef __attribute__((ext_vector_type(2))) __bf16 bf16x2;
typedef __attribute__((ext_vector_type(2))) float f32x2;
typedef __attribute__((ext_vector_type(4))) float f32x4;
typedef __attribute__((ext_vector_type(4))) u32 u32x4;
typedef __attribute__((ext_vector_type(2))) u32 u32x2;
typedef __attribute__((ext_vector_type(4))) short s16x4;
#define LDS __attribute__((address_space(3)))
#define DEV __device__ __forceinline__
#define SB() __builtin_amdgcn_sched_barrier(0)

constexpr int S = 2048, D = 640, F = 1728;
constexpr size_t WS_MOD = 0;
constexpr size_t WS_ZT  = 64 << 10;
constexpr size_t WS_X1  = WS_ZT + (size_t)S * D * 2;
constexpr size_t WS_H2  = WS_X1 + (size_t)S * D * 2;
constexpr size_t WS_W1  = WS_H2 + (size_t)S * D * 2;
constexpr size_t WS_W2  = WS_W1 + (size_t)2 * D * F * 2;
constexpr size_t WS_ACT = WS_W2 + (size_t)D * F * 2;
constexpr size_t WS_ACTL = WS_ACT + (size_t)128 * 48 * 1024;
constexpr size_t WS_FLAG = WS_ACT + (size_t)S * F * 2;
constexpr int FLAG_BYTES = (64 + 512) * 128;
constexpr size_t WS_END = WS_FLAG + FLAG_BYTES;
constexpr size_t WS_STAMP = 64 << 20;

#define AUX_IN 0
#define AUX_ST 0

#define STAMP_DECL
#define STAMP(i)
#define STAMP_OUT(k)

DEV float blo(u32 x) { return __uint_as_float(x << 16); }
DEV float bhi(u32 x) { return __uint_as_float(x & 0xffff0000u); }
DEV u32 pk2(float a, float b) { f32x2 v = {a, b}; return __builtin_bit_cast(u32, __builtin_convertvector(v, bf16x2)); }
DEV f32x4 mfma16(u32x4 a, u32x4 b, f32x4 c) {
  return __builtin_amdgcn_mfma_f32_16x16x32_bf16(__builtin_bit_cast(bf16x8, a), __builtin_bit_cast(bf16x8, b), c, 0, 0, 0);
}
DEV float silu_bf(u32 c) { float x = blo(c); return blo(pk2(x / (1.f + __expf(-x)), 0.f)); }
DEV __amdgpu_buffer_rsrc_t rsrc(const void* p, u32 bytes) { return __builtin_amdgcn_make_buffer_rsrc((void*)p, 0, bytes, 0x00020000); }
DEV float shr1(float x) { return __int_as_float(__builtin_amdgcn_update_dpp(0, __float_as_int(x), 0x138, 0xf, 0xf, true)); }
DEV float xsum(float x) {
  x += __int_as_float(__builtin_amdgcn_update_dpp(0, __float_as_int(x), 0x128, 0xf, 0xf, true));
  auto a = __builtin_amdgcn_permlane16_swap(__float_as_uint(x), __float_as_uint(x), false, false);
  x = __uint_as_float(a[0]) + __uint_as_float(a[1]);
  auto b = __builtin_amdgcn_permlane32_swap(__float_as_uint(x), __float_as_uint(x), false, false);
  return __uint_as_float(b[0]) + __uint_as_float(b[1]);
}
DEV u32x2 trread(LDS char* p) { return __builtin_bit_cast(u32x2, __builtin_amdgcn_ds_read_tr16_b64_v4i16((LDS s16x4*)p)); }
DEV void bar() { asm volatile("s_waitcnt lgkmcnt(0)" ::: "memory"); __builtin_amdgcn_s_barrier(); asm volatile("" ::: "memory"); }

constexpr int KC_COPY = 4160;
constexpr int UR_BYTES = 4608;
constexpr int K1_LDS = 4 * 2 * KC_COPY + 4 * UR_BYTES;

DEV int left_tile(int li) { int q = li / 3; return ((27 * (4 * q + 1 + li - 3 * q)) >> 2) + 6; }

DEV void retile_unit(u32x4 (&v)[4], LDS char* buf, char* dst, int fragstride, bool kperm, int lane) {
#pragma unroll
  for (int i = 0; i < 4; i++) {
    int c = lane + 64 * i, r = c >> 3, ch = c & 7;
    int h = ((r >> 1) & 1) | ((((r >> 2) ^ (r >> 3)) & 1) << 1);
    *(LDS u32x4*)(buf + r * 128 + ((ch ^ (h << 1)) << 4)) = v[i];
  }
  int g = lane >> 4, q = (lane >> 2) & 3, p = lane & 3;
  int r0 = kperm ? 4 * g + q : 8 * g + q, r1 = kperm ? r0 + 16 : r0 + 4;
  int h0 = ((r0 >> 1) & 1) | ((((r0 >> 2) ^ (r0 >> 3)) & 1) << 1);
  int h1 = ((r1 >> 1) & 1) | ((((r1 >> 2) ^ (r1 >> 3)) & 1) << 1);
#pragma unroll
  for (int t = 0; t < 4; t++) {
    u32x2 a = trread(buf + r0 * 128 + ((t ^ h0) << 5) + p * 8);
    u32x2 b = trread(buf + r1 * 128 + ((t ^ h1) << 5) + p * 8);
    u32x4 o = {a.x, a.y, b.x, b.y};
    __builtin_amdgcn_raw_buffer_store_b128(o, rsrc(dst, 4 * fragstride), lane * 16, t * fragstride, AUX_ST);
  }
}

#define PF_STEP 12
#define SIDE_DELAY 4
#define XRN 5
#define AUX_PF 0
#define AUX_G 0
#define AUX_W 0
__global__ __launch_bounds__(512, 2) void k1(const u16* __restrict__ kk, const u16* __restrict__ vv, const u16* __restrict__ shk,
                                             const u16* __restrict__ ssk, const u16* __restrict__ ssd, const u16* __restrict__ cc,
                                             const u16* __restrict__ wmod, const u16* __restrict__ wg, const u16* __restrict__ wu,
                                             const u16* __restrict__ wd, char* __restrict__ ws) {
  __shared__ __attribute__((aligned(16))) char smem[K1_LDS];
  LDS char* sm = (LDS char*)smem;
  const int tid = threadIdx.x, lane = tid & 63, w = __builtin_amdgcn_readfirstlane(tid >> 6);
  const int b = blockIdx.x, xcd = b & 7, bi = b >> 3;
  STAMP_DECL
  STAMP(0);
  auto rk = rsrc(kk, S * D * 2), rv = rsrc(vv, S * D * 2);
  LDS char* urb = sm + 4 * 2 * KC_COPY;
  const int cg = xcd * 20 + bi;
  if (tid < FLAG_BYTES / 256 / 16) *(u32x4*)(ws + WS_FLAG + (b * (FLAG_BYTES / 256 / 16) + tid) * 16) = (u32x4){0, 0, 0, 0};
  auto warm_kv = [&]() {
    u32 pf = 0;
    for (int i = 0; i < w; i++) __builtin_amdgcn_s_sleep(PF_STEP);
    const int pofs = (512 * w + 16 * bi + (lane & 15)) * 1280 + ((160 * xcd) & ~127) + ((lane >> 4) & 1) * 128;
    pf = *(const u32*)((const char*)(lane >= 32 ? vv : kk) + pofs);
    return pf;
  };
  if (w >= 4) {
    if (bi < 20) {
      const int w4 = w - 4;
      constexpr int P = 2;
      u32x2 gk[9], gv[9];
      auto ld = [&](int r) {
        int off = (61 * (4 * r + w4) - 3 + lane) * 1280 + cg * 8;
        gk[r] = __builtin_amdgcn_raw_buffer_load_b64(rk, off, 0, AUX_G);
        gv[r] = __builtin_amdgcn_raw_buffer_load_b64(rv, off, 0, AUX_G);
        SB();
      };
#pragma unroll
      for (int r = 0; r < P; r++) ld(r);
      STAMP(1);
      float cf[4][4];
      {
        const u32x4* sp = (const u32x4*)(shk + cg * 16);
        u32x4 s0 = sp[0], s1 = sp[1];
        cf[0][0] = blo(s0.x); cf[0][1] = bhi(s0.x); cf[0][2] = blo(s0.y); cf[0][3] = bhi(s0.y);
        cf[1][0] = blo(s0.z); cf[1][1] = bhi(s0.z); cf[1][2] = blo(s0.w); cf[1][3] = bhi(s0.w);
        cf[2][0] = blo(s1.x); cf[2][1] = bhi(s1.x); cf[2][2] = blo(s1.y); cf[2][3] = bhi(s1.y);
        cf[3][0] = blo(s1.z); cf[3][1] = bhi(s1.z); cf[3][2] = blo(s1.w); cf[3][3] = bhi(s1.w);
      }
#pragma unroll
      for (int r = 0; r < 9; r++) {
        int row = 61 * (4 * r + w4) - 3 + lane;
        float kf[4] = {blo(gk[r].x), bhi(gk[r].x), blo(gk[r].y), bhi(gk[r].y)};
        float vf[4] = {blo(gv[r].x), bhi(gv[r].x), blo(gv[r].y), bhi(gv[r].y)};
        float u[4];
#pragma unroll
        for (int c = 0; c < 4; c++) {
          float t = kf[c] * cf[c][3];
          t = shr1(t) + kf[c] * cf[c][2];
          t = shr1(t) + kf[c] * cf[c][1];
          t = shr1(t) + kf[c] * cf[c][0];
          u[c] = t * vf[c];
        }
        u32 p01 = pk2(u[0], u[1]), p23 = pk2(u[2], u[3]);
        if (lane >= 3 && row < S) {
          LDS u16* up = (LDS u16*)(urb + (2047 - row) * 2);
          up[0] = (u16)p01; up[UR_BYTES / 2] = (u16)(p01 >> 16);
          up[UR_BYTES] = (u16)p23; up[3 * UR_BYTES / 2] = (u16)(p23 >> 16);
        }
        STAMP(2 + r);
        bar();
        SB();
        if (r + P < 9) ld(r + P);
      }
      STAMP(15); STAMP_OUT(1);
      return;
    }
  } else if (bi < 20) {
    const int d = cg * 4 + w;
    auto rs = rsrc(ssk, S * D * 2);
    u32x4 kw[4];
#pragma unroll
    for (int i = 0; i < 4; i++) kw[i] = __builtin_amdgcn_raw_buffer_load_b128(rs, d * 4096 + (lane + 64 * i) * 16, 0, AUX_IN);
    SB();
    const u32 pf = warm_kv();
    SB();
    const u32 ddw = ((const u32*)ssd)[d >> 1];
    const float dd = (d & 1) ? bhi(ddw) : blo(ddw);
    LDS char* kc = sm + w * 2 * KC_COPY;
    LDS char* ur = urb + w * UR_BYTES;
    const u32x4 z4 = {0, 0, 0, 0};
    if (lane < 32) *(LDS u32x4*)(ur + 4096 + lane * 16) = z4;
#pragma unroll
    for (int i = 0; i < 4; i++) {
      u32 nx0 = i < 3 ? __builtin_amdgcn_readfirstlane(kw[i < 3 ? i + 1 : i].x) : 0u;
      u32 nx = __builtin_amdgcn_update_dpp(nx0, kw[i].x, 0x130, 0xf, 0xf, false);
      u32x4 o = {__builtin_amdgcn_alignbit(kw[i].y, kw[i].x, 16), __builtin_amdgcn_alignbit(kw[i].z, kw[i].y, 16),
                 __builtin_amdgcn_alignbit(kw[i].w, kw[i].z, 16), __builtin_amdgcn_alignbit(nx, kw[i].w, 16)};
      *(LDS u32x4*)(kc + 32 + (lane + 64 * i) * 16) = kw[i];
      *(LDS u32x4*)(kc + KC_COPY + 32 + (lane + 64 * i) * 16) = o;
    }
    if (lane == 0) {
      u32x4 e = {0, 0, 0, kw[0].x << 16};
      *(LDS u32x4*)(kc) = z4; *(LDS u32x4*)(kc + 16) = z4;
      *(LDS u32x4*)(kc + KC_COPY) = z4; *(LDS u32x4*)(kc + KC_COPY + 16) = e;
    }
    const int a = lane & 15, g = lane >> 4;
    LDS const u32* ap = (LDS const u32*)(kc + ((a & 1) ? 0 : KC_COPY)) + 4 * g + ((a + 1) >> 1);
    LDS const u32* ab[4];
#pragma unroll
    for (int i = 0; i < 4; i++) { ab[i] = ap + 256 * i; asm volatile("" : "+v"(ab[i])); }
    auto rdA = [&](int m) { LDS const u32* p = ab[m >> 4] + 16 * (m & 15); return (u32x4){p[0], p[1], p[2], p[3]}; };
    constexpr int XR = XRN;
    u32x4 A[8 * XR];
#pragma unroll
    for (int m = 0; m < 8 * XR; m++) A[m] = rdA(m);
    STAMP(1);
    f32x4 acc[8];
#pragma unroll
    for (int n = 0; n < 8; n++) acc[n] = (f32x4){0, 0, 0, 0};
    LDS char* bp = ur + (2032 - 16 * a + 8 * g) * 2;
    auto rz = rsrc(ws + WS_ZT + (size_t)d * 4096, 4096);
    u32x4 As[2][8 - XR], B[2];
    u32x2 uu;
    auto zout = [&](int y) {
      u32x2 o = {pk2(acc[y][0] + bhi(uu.y) * dd, acc[y][1] + blo(uu.y) * dd), pk2(acc[y][2] + bhi(uu.x) * dd, acc[y][3] + blo(uu.x) * dd)};
      __builtin_amdgcn_raw_buffer_store_b64(o, rz, (16 * a + 4 * g) * 2, 512 * y, AUX_ST);
    };
#pragma unroll
    for (int x = XR; x < 8; x++) As[1][x - XR] = rdA(8 * x + 7);
    bar();
    B[1] = *(LDS u32x4*)(bp + 32 * 7 * 2);
#pragma unroll
    for (int y = 0; y < 8; y++) {
#pragma unroll
      for (int rho = 7; rho >= 0; rho--) {
        const int yn = rho ? y : y + 1, rn = (rho + 7) & 7;
        if (rho == 3) { __builtin_amdgcn_s_barrier(); asm volatile("" ::: "memory"); }
        if (yn < 8) B[rn & 1] = *(LDS u32x4*)(bp + (32 * rn - 256 * yn) * 2);
#pragma unroll
        for (int x = XR; x < 8; x++)
          if (x < 8 - yn) As[rn & 1][x - XR] = rdA(8 * x + rn);
        SB();
#pragma unroll
        for (int x = 0; x < 8 - y; x++) acc[x + y] = mfma16(x < XR ? A[x < XR ? 8 * x + rho : 0] : As[rho & 1][x < XR ? 0 : x - XR], B[rho & 1], acc[x + y]);
        SB();
        if (rho == 6 && y > 0) zout(y - 1);
        if (rho == 2) uu = *(LDS u32x2*)(ur + (2044 - 256 * y - 16 * a - 4 * g) * 2);
        SB();
      }
      STAMP(2 + y);
    }
    zout(7);
    asm volatile("" ::"v"(pf));
    STAMP(15); STAMP_OUT(0);
    return;
  }
  {
    const int sid = (bi - 20) * 8 + xcd;
    u32 pf = 0;
    if (w < 4) pf = warm_kv();
    for (int i = w < 4 ? w : 0; i < SIDE_DELAY; i++) __builtin_amdgcn_s_sleep(PF_STEP);
    int u0, nu;
    if (sid < 60) {
      u0 = sid * 9; nu = 9;
      auto rw = rsrc(wmod, D * 6 * D * 2);
      const int ch = tid & 7, rg = tid >> 3;
      u32x4 wv[10];
      u16 c0 = cc[tid], c1 = cc[512 + (tid & 127)];
      SB();
#pragma unroll
      for (int i = 0; i < 10; i++) wv[i] = __builtin_amdgcn_raw_buffer_load_b128(rw, (rg + 64 * i) * 7680 + sid * 128 + ch * 16, 0, AUX_IN);
      LDS float* sil = (LDS float*)(sm + 32768);
      SB();
      sil[tid] = silu_bf(c0); sil[512 + (tid & 127)] = silu_bf(c1);
      bar();
      float ac[8] = {0, 0, 0, 0, 0, 0, 0, 0};
#pragma unroll
      for (int i = 0; i < 10; i++) {
        float s = sil[rg + 64 * i];
        ac[0] += s * blo(wv[i].x); ac[1] += s * bhi(wv[i].x); ac[2] += s * blo(wv[i].y); ac[3] += s * bhi(wv[i].y);
        ac[4] += s * blo(wv[i].z); ac[5] += s * bhi(wv[i].z); ac[6] += s * blo(wv[i].w); ac[7] += s * bhi(wv[i].w);
      }
#pragma unroll
      for (int j = 0; j < 8; j++) ac[j] = xsum(ac[j]);
      LDS float* red = (LDS float*)(sm + 36864);
      if (lane < 8) {
        *(LDS f32x4*)(red + w * 64 + lane * 8) = (f32x4){ac[0], ac[1], ac[2], ac[3]};
        *(LDS f32x4*)(red + w * 64 + lane * 8 + 4) = (f32x4){ac[4], ac[5], ac[6], ac[7]};
      }
      bar();
      if (tid < 64) {
        float t = 0;
#pragma unroll
        for (int i = 0; i < 8; i++) t += red[i * 64 + tid];
        ((u16*)(ws + WS_MOD))[sid * 64 + tid] = (u16)pk2(t, 0.f);
      }
    } else {
      u0 = 540 + (sid - 60) * 30; nu = 30;
    }
    STAMP(1);
    LDS char* buf = sm + w * 4096;
    u32x4 v[4][4];
    char* dst[4]; int fstr[4];
#pragma unroll
    for (int jj = 0; jj < 4; jj++) {
      int u = u0 + w + 8 * jj;
      if (w + 8 * jj < nu) {
        const char* src; int pitch, hi16 = 16;
        if (u < 1080) {
          int mat = u >= 540, uu = u - 540 * mat, ks = uu / 27, fq = uu - 27 * ks;
          src = (const char*)(mat ? wu : wg) + ks * 32 * 3456 + fq * 128; pitch = 3456;
          dst[jj] = ws + WS_W1 + ((size_t)(mat * 108 + fq * 4) * 20 + ks) * 1024; fstr[jj] = 20 * 1024;
        } else {
          int uu = u - 1080, ks = uu / 10, nq = uu - 10 * ks, fa;
          if (ks < 48) fa = ((27 * (ks & 15)) >> 2) + 2 * (ks >> 4);
          else { int l0 = 2 * (ks - 48), l1 = l0 + 1; fa = left_tile(l0); hi16 = 16 * (left_tile(l1) - fa); }
          src = (const char*)wd + fa * 16 * 1280 + nq * 128; pitch = 1280;
          dst[jj] = ws + WS_W2 + ((size_t)(nq * 4) * 54 + ks) * 1024; fstr[jj] = 54 * 1024;
        }
        auto rr = rsrc(src, (hi16 + 16) * pitch);
#pragma unroll
        for (int i = 0; i < 4; i++) v[jj][i] = __builtin_amdgcn_raw_buffer_load_b128(rr, (lane >> 3) * pitch + (lane & 7) * 16, (8 * (i & 1) + (i >> 1) * hi16) * pitch, AUX_W);
      }
    }
#pragma unroll
    for (int jj = 0; jj < 4; jj++)
      if (w + 8 * jj < nu) retile_unit(v[jj], buf, dst[jj], fstr[jj], fstr[jj] != 20 * 1024, lane);
    asm volatile("" ::"v"(pf));
  }
  STAMP(15); if (w < 4) STAMP_OUT(0);
}

constexpr int RING = 7 * 20 * 1024, KF_ZB = RING, KF_RED = KF_ZB + 10240, KF_DONE = KF_RED + 1280, KF_LDS = KF_DONE + 16;
constexpr int SC1 = 16;
typedef __attribute__((address_space(1))) u32 gu32;
DEV void set_flag(char* p) { __hip_atomic_store((gu32*)p, 1u, __ATOMIC_RELAXED, __HIP_MEMORY_SCOPE_AGENT); }
DEV u32 peek_flags(char* p, int n, int lane) { return __hip_atomic_load((gu32*)p + (lane & (n - 1)), __ATOMIC_RELAXED, __HIP_MEMORY_SCOPE_AGENT); }
DEV void wait_flags(char* p, int n, int lane) {
  gu32* q = (gu32*)p + (lane & (n - 1));
#pragma unroll 1
  for (int spin = 0; spin < (1 << 20); spin++) {
    if (__all(__hip_atomic_load(q, __ATOMIC_RELAXED, __HIP_MEMORY_SCOPE_AGENT) != 0)) break;
    __builtin_amdgcn_s_sleep(1);
  }
  asm volatile("" ::: "memory");
}

DEV float dot2(u32 a, u32 b, float c) { return __builtin_amdgcn_fdot2_f32_bf16(__builtin_bit_cast(bf16x2, a), __builtin_bit_cast(bf16x2, b), c, false); }
DEV void row_stats(u32x4 v, LDS char* dst, int lane) {
  const u32 one = 0x3f803f80u;
  float s = dot2(v.w, one, dot2(v.z, one, dot2(v.y, one, dot2(v.x, one, 0.f))));
  float q = dot2(v.w, v.w, dot2(v.z, v.z, dot2(v.y, v.y, dot2(v.x, v.x, 0.f))));
  auto a = __builtin_amdgcn_permlane32_swap(__float_as_uint(s), __float_as_uint(q), false, false);
  float x = __uint_as_float(a[0]) + __uint_as_float(a[1]);
  auto b = __builtin_amdgcn_permlane16_swap(__float_as_uint(x), __float_as_uint(x), false, false);
  x = __uint_as_float(b[0]) + __uint_as_float(b[1]);
  x += __int_as_float(__builtin_amdgcn_update_dpp(0, __float_as_int(x), 0x128, 0xf, 0xf, true));
  if ((lane & 24) == 0) *(LDS float*)(dst + (lane & 7) * 8 + (lane >> 5) * 4) = x;
}
DEV f32x2 mean_rstd(LDS char* red, int t) {
  f32x2 a = {0, 0};
#pragma unroll
  for (int i = 0; i < 10; i++) a += *(LDS f32x2*)(red + i * 64 + t * 8);
  float mean = a.x * (1.f / D), rstd = __builtin_amdgcn_rsqf(fmaxf(a.y * (1.f / D) - mean * mean, 0.f) + 1e-5f);
  return (f32x2){rstd, -mean * rstd};
}
DEV void unpack8(u32x4 v, f32x2 (&f)[4]) {
  f[0] = (f32x2){blo(v.x), bhi(v.x)}; f[1] = (f32x2){blo(v.y), bhi(v.y)}; f[2] = (f32x2){blo(v.z), bhi(v.z)}; f[3] = (f32x2){blo(v.w), bhi(v.w)};
}
DEV u32x4 pack8(f32x2 (&f)[4]) { return (u32x4){pk2(f[0].x, f[0].y), pk2(f[1].x, f[1].y), pk2(f[2].x, f[2].y), pk2(f[3].x, f[3].y)}; }

template <int NI>
DEV void ln_part(const u16* __restrict__ qq, char* __restrict__ ws, LDS char* sm, int s0, int w, int lane, int pfo
) {
  const int t = lane & 7, s = s0 + t;
  u32x4 zr[NI], qv[NI], msa[NI], mca[NI], mga[NI], msb[NI], mcb[NI];
  int wi[NI], c[NI];
#pragma unroll
  for (int i = 0; i < NI; i++) {
    wi[i] = i == 0 ? w : w + 2;
    c[i] = wi[i] * 8 + (lane >> 3);
    qv[i] = *(const u32x4*)(qq + s * D + 8 * c[i]);
    const u32x4* mp = (const u32x4*)(ws + WS_MOD) + c[i];
    msa[i] = mp[0]; mca[i] = mp[80]; mga[i] = mp[160]; msb[i] = mp[240]; mcb[i] = mp[320];
  }
  SB();
#pragma unroll
  for (int i = 0; i < NI; i++) zr[i] = *(const u32x4*)(ws + WS_ZT + (size_t)(wi[i] * 64 + lane) * 4096 + s0 * 2);
  SB();
  f32x2 x[NI][4], sa[NI][4], ca[NI][4], ga[NI][4];
#pragma unroll
  for (int i = 0; i < NI; i++) {
    row_stats(qv[i], sm + KF_RED + wi[i] * 64, lane);
    unpack8(qv[i], x[i]); unpack8(msa[i], sa[i]); unpack8(mca[i], ca[i]); unpack8(mga[i], ga[i]);
  }
  SB();
#pragma unroll
  for (int i = 0; i < NI; i++) *(LDS u32x4*)(sm + KF_ZB + wi[i] * 1024 + lane * 16) = zr[i];
  STAMP(11);
  bar();
  u32 pf = 0;
  pf = __builtin_amdgcn_raw_buffer_load_b32(rsrc(ws + WS_W1, 2 * D * F * 2), pfo, 0, 0);
  STAMP(12);
  f32x2 mr = mean_rstd(sm + KF_RED, t);
  auto rx = rsrc(ws + WS_X1, S * D * 2), rh = rsrc(ws + WS_H2, S * D * 2);
  u32x4 x1[NI];
#pragma unroll
  for (int i = 0; i < NI; i++) {
    f32x2 z[4], p[4];
    int i16 = lane & 15, G = lane >> 4, q = i16 >> 2, pp = i16 & 3;
    LDS char* a = sm + KF_ZB + wi[i] * 1024 + (16 * G + 8 * (pp >> 1) + q) * 16 + (pp & 1) * 8;
    u32x2 z0 = trread(a), z1 = trread(a + 64);
    unpack8((u32x4){z0.x, z0.y, z1.x, z1.y}, z);
#pragma unroll
    for (int j = 0; j < 4; j++) { f32x2 n = x[i][j] * mr.x + mr.y, h = n * ca[i][j] + (n + sa[i][j]); p[j] = ga[i][j] * (h * z[j]) + x[i][j]; }
    x1[i] = pack8(p);
    row_stats(x1[i], sm + KF_RED + 640 + wi[i] * 64, lane);
    __builtin_amdgcn_raw_buffer_store_b128(x1[i], rx, (s * D + 8 * c[i]) * 2, 0, SC1);
    unpack8(x1[i], x[i]); unpack8(msb[i], sa[i]); unpack8(mcb[i], ca[i]);
  }
  bar();
  STAMP(13);
  mr = mean_rstd(sm + KF_RED + 640, t);
#pragma unroll
  for (int i = 0; i < NI; i++) {
    f32x2 o[4];
#pragma unroll
    for (int j = 0; j < 4; j++) { f32x2 n = x[i][j] * mr.x + mr.y; o[j] = n * ca[i][j] + (n + sa[i][j]); }
    __builtin_amdgcn_raw_buffer_store_b128(pack8(o), rh, (((s >> 4) * 20 + (c[i] >> 2)) * 64 + (c[i] & 3) * 16 + (s & 15)) * 16, 0, SC1);
  }
  STAMP(14);
  asm volatile("s_waitcnt vmcnt(0)" ::"v"(pf) : "memory");
}

#define PF2K 4
#define LASTBAR 9
#define HD 8
__global__ __launch_bounds__(512, 2) void kf(const u16* __restrict__ qq, char* __restrict__ ws, u16* __restrict__ out) {
  extern __shared__ __attribute__((aligned(16))) char smem[];
  LDS char* sm = (LDS char*)smem;
  const int tid = threadIdx.x, lane = tid & 63, w = __builtin_amdgcn_readfirstlane(tid >> 6);
  const int xcd = blockIdx.x & 7, bi = blockIdx.x >> 3;
  const int M = 8 * (xcd >> 2) + (bi & 7), fs = 4 * (xcd & 3) + (bi >> 3), ft0 = (27 * fs) >> 2;
  const int hm = fs >> 3, nt0 = (fs & 7) * 5, mg = M * 8 + hm * 4;
  char* flagH = ws + WS_FLAG + M * 4 * 128;
  char* flagA = ws + WS_FLAG + 64 * 128 + M * 32 * 128;
  STAMP_DECL
  STAMP(0);
  if (tid == 0) *(LDS u32*)(sm + KF_DONE) = 0;
#define STARG
  const int pl = bi * 270 + tid, pfo = tid < 270 ? ((pl >= 4320 ? 108 * 160 - 4320 : 0) + (xcd & 3) * 4320 + pl) * 128 : -1;
  if (w >= 6) ln_part<2>(qq, ws, sm, M * 128 + fs * 8, w, lane, pfo STARG); else ln_part<1>(qq, ws, sm, M * 128 + fs * 8, w, lane, pfo STARG);
  __builtin_amdgcn_s_barrier();
  if (tid == 0) set_flag(flagH + (fs >> 2) * 128 + (fs & 3) * 4);
  STAMP(1);

  auto ra = rsrc(ws + WS_ACT, S * F * 2);
  auto rw2 = rsrc(ws + WS_W2 + (size_t)nt0 * 54 * 1024, 5 * 54 * 1024);
  f32x4 oc[5][4];
  auto dmm = [&](u32x4 (&A)[4], u32x4 (&W)[5]) {
#pragma unroll
    for (int n = 0; n < 5; n++)
#pragma unroll
      for (int m = 0; m < 4; m++) oc[n][m] = mfma16(W[n], A[m], oc[n][m]);
  };
  auto ldW2 = [&](u32x4 (&W)[5], int kq) {
#pragma unroll
    for (int n = 0; n < 5; n++) W[n] = __builtin_amdgcn_raw_buffer_load_b128(rw2, lane * 16, (n * 54 + kq) * 1024, 0);
  };
  auto ldA = [&](u32x4 (&A)[4], int kq) {
#pragma unroll
    for (int m = 0; m < 4; m++) A[m] = __builtin_amdgcn_raw_buffer_load_b128(ra, lane * 16, ((mg + m) * 48 + kq) * 1024, SC1);
  };

  if (w >= 4) {
    const int j = w - 4;
    auto rr = rsrc(ws + WS_W1 + (size_t)ft0 * 20 * 1024, 2 * D * F * 2 - ft0 * 20 * 1024);
    const int v1 = j * 1024 + lane * 16, v2 = ((j & 1) * 108 * 20 + (j >> 1) * 5) * 1024 + lane * 16;
    LDS char* l1 = sm + j * 6 * 1024;
    LDS char* l2 = sm + ((j >> 1) * 10 + (j & 1)) * 1024;
    auto issue = [&](int g) {
      if (g < 5) {
#pragma unroll
        for (int e = 0; e < 6; e++)
          __builtin_amdgcn_raw_ptr_buffer_load_lds(rr, (LDS void*)(l1 + (24 * g + e) * 1024), 16, v1, (((e & 1) * 108 + (e >> 1)) * 20 + 4 * g) * 1024, 0, 0);
      } else {
#pragma unroll
        for (int e = 0; e < 5; e++)
          __builtin_amdgcn_raw_ptr_buffer_load_lds(rr, (LDS void*)(l2 + ((20 * g + 20) % 140 + 2 * e) * 1024), 16, v2, ((3 + ((g - 5) >> 1)) * 20 + ((g - 5) & 1) * 10 + e) * 1024, 0, 0);
      }
    };
#define WV(n) asm volatile("s_waitcnt vmcnt(" #n ")" ::: "memory")
#define BR() __builtin_amdgcn_s_barrier()
    issue(0); issue(1); issue(2);
    STAMP(2);
    WV(12); BR();
    STAMP(3);
    issue(3); issue(4); issue(5);
    WV(23); BR();
    WV(17); BR(); issue(6); issue(7);
    WV(21); BR(); issue(8);
    WV(20); BR(); issue(9);
    WV(20); BR(); issue(10);
    {
      auto r2 = rsrc(ws + WS_W2 + (size_t)(xcd & 1) * 20 * 54 * 1024, 20 * 54 * 1024);
      __builtin_amdgcn_raw_ptr_buffer_load_lds(r2, (LDS void*)(sm + KF_ZB), 4, (bi * 270 + j * 68 + lane) * 128, 0, 0, 0);
      __builtin_amdgcn_raw_ptr_buffer_load_lds(r2, (LDS void*)(sm + KF_ZB), 4, lane < 4 ? (bi * 270 + j * 68 + 64 + lane) * 128 : -1, 0, 0, 0);
    }
    WV(22); BR(); issue(11);
    WV(22); BR(); issue(12);
    WV(20); BR();
    __builtin_amdgcn_s_waitcnt(0x0f70);
    wait_flags(flagA + ((hm * 4 + 0) * 4 + j) * 128, 8, lane);
    BR();
    STAMP(4);
#pragma unroll
    for (int n = 0; n < 5; n++)
#pragma unroll
      for (int m = 0; m < 4; m++) oc[n][m] = (f32x4){0, 0, 0, 0};
    u32x4 A[4][4], W[4][5];
    char* f1 = flagA + ((hm * 4 + 1) * 4 + j) * 128;
#pragma unroll
    for (int i = 0; i < 3; i++) { ldA(A[i], 4 * j + i); ldW2(W[i], 4 * j + i); SB(); }
    u32 early = peek_flags(f1, 8, lane);
    SB();
    ldA(A[3], 4 * j + 3); ldW2(W[3], 4 * j + 3); SB();
    STAMP(5);
    dmm(A[0], W[0]); SB();
    if (!__all(early != 0)) wait_flags(f1, 8, lane);
    STAMP(6);
#pragma unroll
    for (int i = 0; i < 4; i++) { if (i) dmm(A[i], W[i]); SB(); ldA(A[i], 16 + 4 * j + i); ldW2(W[i], 16 + 4 * j + i); SB(); }
#pragma unroll
    for (int i = 0; i < 4; i++) { dmm(A[i], W[i]); SB(); }
    STAMP(7);
    while (*(volatile LDS u32*)(sm + KF_DONE) < 4) __builtin_amdgcn_s_sleep(1);
#pragma unroll
    for (int n = 0; n < 5; n++)
#pragma unroll
      for (int m = 0; m < 4; m++) *(LDS f32x4*)(sm + (j * 20 + n * 4 + m) * 1024 + lane * 16) = oc[n][m];
    bar();
    STAMP(8);
    __builtin_amdgcn_s_barrier();
    STAMP(15); STAMP_OUT(3);
    return;
  }
  __builtin_amdgcn_s_setprio(2);
  {
    u32x4 H[20][2], W[8];
    f32x4 acc[3][2][2];
    auto rh = rsrc(ws + WS_H2 + (size_t)(M * 8 + 2 * w) * 20 * 1024, 40 * 1024);
    auto ldH = [&](int ks) {
      H[ks][0] = __builtin_amdgcn_raw_buffer_load_b128(rh, lane * 16, ks * 1024, SC1);
      H[ks][1] = __builtin_amdgcn_raw_buffer_load_b128(rh, lane * 16, (20 + ks) * 1024, SC1);
    };
    wait_flags(flagH + w * 128, 4, lane);
    STAMP(2);
#pragma unroll
    for (int ks = 0; ks < HD; ks++) ldH(ks);
    SB();
    LDS char* base = sm + lane * 16;
    auto rdW = [&](int i) {
      if (i < 120 ? i % 24 == 0 : i % 20 == 0 && i / 20 - 1 <= LASTBAR) { __builtin_amdgcn_s_barrier(); asm volatile("" ::: "memory"); }
      W[i & 7] = *(LDS u32x4*)(base + (i % 140) * 1024);
    };
    const f32x4 z4 = {0, 0, 0, 0};
#pragma unroll
    for (int i = 0; i < 6; i++) rdW(i);
    STAMP(3);
#pragma unroll
    for (int i = 0; i < 120; i++) {
      const int ks = i / 6, t = (i % 6) >> 1, mat = i & 1;
      rdW(i + 6);
      if (i % 6 == 0 && ks + HD < 20) ldH(ks + HD);
      SB();
      acc[t][mat][0] = mfma16(W[i & 7], H[ks][0], ks ? acc[t][mat][0] : z4);
      acc[t][mat][1] = mfma16(W[i & 7], H[ks][1], ks ? acc[t][mat][1] : z4);
      SB();
    }
    STAMP(4);
    const bool seven = (fs & 3) != 0;
    u32 pk[2][4];
    auto relu = [](float x) { return __int_as_float(max(__float_as_int(x), 0)); };
    auto quarter = [&](int sl, int q, int odd) {
      const int m = q >> 1, h = 2 * (q & 1);
      pk[m][2 * odd + (q & 1)] = pk2(relu(acc[sl][0][m][h]) * acc[sl][1][m][h], relu(acc[sl][0][m][h + 1]) * acc[sl][1][m][h + 1]);
    };
    auto store_pk = [&](int p, int m) {
      __builtin_amdgcn_raw_buffer_store_b128((u32x4){pk[m][0], pk[m][1], pk[m][2], pk[m][3]}, ra, lane * 16, ((M * 8 + 2 * w + m) * 48 + 16 * p + fs) * 1024, SC1);
    };
    const int fw = (fs & 3) * 2 + (w & 1);
    char* myflag = flagA + ((w >> 1) * 16 + (fs >> 2)) * 128 + fw * 4;
    auto publish = [&](char* f) {
      asm volatile("s_waitcnt vmcnt(0)" ::: "memory");
      if (lane == 0) set_flag(f);
    };
    auto tile = [&](int t, int sl, auto&& hook) {
#pragma unroll
      for (int jj = 0; jj < 40; jj++) {
        const int i = 120 + 40 * (t - 3) + jj, ks = jj >> 1, mat = jj & 1;
        if (i + 6 < 280) rdW(i + 6);
        SB();
        acc[sl][mat][0] = mfma16(W[i & 7], H[ks][0], ks ? acc[sl][mat][0] : z4);
        acc[sl][mat][1] = mfma16(W[i & 7], H[ks][1], ks ? acc[sl][mat][1] : z4);
        SB();
        hook(jj);
        SB();
      }
    };
    auto odd_hook = [&](int jj, int sl, int p) {
      if (jj >= 2 && jj <= 8 && !(jj & 1)) quarter(sl, (jj - 2) >> 1, 1);
      if (jj == 5) store_pk(p, 0);
      if (jj == 9) store_pk(p, 1);
      if (jj == 38) publish(myflag + p * 512);
    };
    auto even_hook = [&](int jj, int sl, int j0) { if (jj >= j0 && jj < j0 + 8 && !(jj & 1)) quarter(sl, (jj - j0) >> 1, 0); };
#pragma unroll
    for (int q = 0; q < 4; q++) quarter(0, q, 0);
    tile(3, 0, [&](int jj) { odd_hook(jj, 1, 0); even_hook(jj, 2, 12); });
    tile(4, 1, [&](int jj) { odd_hook(jj, 0, 1); });
    tile(5, 0, [&](int jj) { even_hook(jj, 1, 2); });
    char* lastflag;
    if (seven) {
      tile(6, 1, [&](int jj) { odd_hook(jj, 0, 2); });
      const int li = 3 * (fs >> 2) + (fs & 3) - 1;
#pragma unroll
      for (int q = 0; q < 4; q++) quarter(1, q, 0);
      __builtin_amdgcn_raw_buffer_store_b128((u32x4){pk[0][0], pk[0][1], pk[1][0], pk[1][1]}, ra, lane * 16, (int)(WS_ACTL - WS_ACT) + ((M * 4 + w) * 12 + li) * 1024, SC1);
      lastflag = flagA + (((w >> 1) * 4 + 3) * 4 + ((li >> 1) & 3)) * 128 + ((li >> 3) * 4 + (li & 1) * 2 + (w & 1)) * 4;
    } else {
#pragma unroll
      for (int q = 0; q < 4; q++) quarter(0, q, 1);
      store_pk(2, 0); store_pk(2, 1);
      lastflag = myflag + 2 * 512;
    }
    STAMP(5);
    if (lane == 0) __hip_atomic_fetch_add((LDS u32*)(sm + KF_DONE), 1u, __ATOMIC_RELAXED, __HIP_MEMORY_SCOPE_WORKGROUP);
    publish(lastflag);
  }
  STAMP(6);
  {
    u32x4 A[4][4], W[4][5], WL[2][5], L[2][4];
    char* f2 = flagA + ((hm * 4 + 2) * 4 + w) * 128;
    char* f3 = flagA + ((hm * 4 + 3) * 4 + w) * 128;
    wait_flags(f2, 8, lane);
#pragma unroll
    for (int i = 0; i < 4; i++) { ldA(A[i], 32 + 4 * w + i); ldW2(W[i], 32 + 4 * w + i); SB(); }
    STAMP(7);
#pragma unroll
    for (int n = 0; n < 5; n++)
#pragma unroll
      for (int m = 0; m < 4; m++) oc[n][m] = (f32x4){0, 0, 0, 0};
    dmm(A[0], W[0]); SB();
    u32 early = peek_flags(f3, w < 2 ? 8 : 4, lane); SB();
    dmm(A[1], W[1]); SB();
    ldW2(WL[0], 48 + w); ldW2(WL[1], w < 2 ? 52 + w : 0x10000); SB();
    dmm(A[2], W[2]); dmm(A[3], W[3]); SB();
    const int tok = (mg + w) * 16 + (lane & 15), d0 = nt0 * 16 + (lane >> 4) * 4;
    u32x2 xr[5], gb[5];
    auto rx = rsrc(ws + WS_X1, S * D * 2);
    STAMP(8);
    if (!__all(early != 0)) wait_flags(f3, w < 2 ? 8 : 4, lane);
    STAMP(9);
#pragma unroll
    for (int i = 0; i < 2; i++)
#pragma unroll
      for (int e = 0; e < 4; e++)
        L[i][e] = __builtin_amdgcn_raw_buffer_load_b128(ra, lane * 16, i && w >= 2 ? 0x40000000 : (int)(WS_ACTL - WS_ACT) + ((M * 4 + hm * 2 + (e >> 1)) * 12 + 2 * (w + 4 * i) + (e & 1)) * 1024, SC1);
#pragma unroll
    for (int n = 0; n < 5; n++) {
      xr[n] = __builtin_amdgcn_raw_buffer_load_b64(rx, (tok * D + d0 + 16 * n) * 2, 0, SC1);
      gb[n] = *(const u32x2*)(ws + WS_MOD + (5 * D + d0 + 16 * n) * 2);
    }
    SB();
    __builtin_amdgcn_s_barrier();
    asm volatile("" ::: "memory");
#pragma unroll
    for (int n = 0; n < 5; n++)
#pragma unroll
      for (int m = 0; m < 4; m++) oc[n][m] += *(LDS f32x4*)(sm + (w * 20 + n * 4 + m) * 1024 + lane * 16);
    SB();
#pragma unroll
    for (int i = 0; i < 2; i++) {
      u32x4 AL[4] = {{L[i][0].x, L[i][0].y, L[i][1].x, L[i][1].y}, {L[i][0].z, L[i][0].w, L[i][1].z, L[i][1].w},
                     {L[i][2].x, L[i][2].y, L[i][3].x, L[i][3].y}, {L[i][2].z, L[i][2].w, L[i][3].z, L[i][3].w}};
      dmm(AL, WL[i]);
    }
    STAMP(10);
    LDS char* rb = sm + 80 * 1024 + lane * 16;
#pragma unroll
    for (int m = 0; m < 4; m++) {
      if (m != w) {
        int slot = (w - m - 1) & 3;
#pragma unroll
        for (int n = 0; n < 5; n++) *(LDS f32x4*)(rb + ((m * 3 + slot) * 5 + n) * 1024) = oc[n][m];
      }
    }
    bar();
    auto fin = [&](int m) {
#pragma unroll
      for (int n = 0; n < 5; n++) {
        f32x4 a = oc[n][m];
#pragma unroll
        for (int sl = 0; sl < 3; sl++) a += *(LDS f32x4*)(rb + ((m * 3 + sl) * 5 + n) * 1024);
        u32x2 o = {pk2(blo(xr[n].x) + blo(gb[n].x) * a[0], bhi(xr[n].x) + bhi(gb[n].x) * a[1]),
                   pk2(blo(xr[n].y) + blo(gb[n].y) * a[2], bhi(xr[n].y) + bhi(gb[n].y) * a[3])};
        *(u32x2*)(out + (size_t)tok * D + d0 + 16 * n) = o;
      }
    };
    if (w == 0) fin(0); else if (w == 1) fin(1); else if (w == 2) fin(2); else fin(3);
  }
  STAMP(15); STAMP_OUT(2);
}

extern "C" void kernel_launch(void* const* d_in, const int* in_sizes, int n_in, void* d_out, int out_size, void* d_ws, size_t ws_size,
                              hipStream_t stream) {
  const u16* q = (const u16*)d_in[0];
  char* ws = (char*)d_ws;
  (void)hipFuncSetAttribute((const void*)kf, hipFuncAttributeMaxDynamicSharedMemorySize, KF_LDS);
  k1<<<256, 512, 0, stream>>>((const u16*)d_in[1], (const u16*)d_in[2], (const u16*)d_in[3], (const u16*)d_in[4], (const u16*)d_in[5],
                              (const u16*)d_in[10], (const u16*)d_in[11], (const u16*)d_in[7], (const u16*)d_in[8], (const u16*)d_in[9], ws);
  kf<<<256, 512, KF_LDS, stream>>>(q, ws, (u16*)d_out);
}
```
